```python
import torch

def _softmax(x, axis=-1):
    return torch.softmax(x, dim=axis)
def _gelu(x):
    return torch.nn.functional.gelu(x, approximate="tanh")
def _silu(x):
    return torch.nn.functional.silu(x)
def _sigmoid(x):
    return torch.sigmoid(x)
def _layernorm(x, axis=-1, eps=1e-5):
    m = x.mean(dim=axis, keepdim=True)
    v = x.var(dim=axis, keepdim=True, unbiased=False)
    return (x - m) / torch.sqrt(v + eps)
def _scatter_add(tgt, idx, src, axis=0):
    return tgt.clone().index_add_(axis, idx, src)
def _segment_sum(src, seg, n):
    out = torch.zeros((n,) + src.shape[1:], dtype=src.dtype)
    return out.index_add_(0, seg, src)
def _pad(x, pw):
    flat = tuple(v for pair in reversed(pw) for v in pair)
    return torch.nn.functional.pad(x, flat)

kernel_name = 'standalone__sliding_block_cached__sampling'

def setup_inputs():

    _w = (lambda a: (lambda t: t.to(torch.bfloat16) if t.is_floating_point() else t)(torch.as_tensor(a)))
    B, Sq, D = 8, 32, 1024
    Hq, Hkv, Sk, Dh = 32, 4, 8192, 128
    assert Hq % Hkv == 0, (Hq, Hkv)
    return {
        "x": _w((lambda *s: torch.randn(*s, dtype=torch.bfloat16) * 0.8)(B, Sq, D)),
        "k_cache": _w((lambda *s: torch.randn(*s, dtype=torch.bfloat16) * 0.8)(B, Hkv, Sk, Dh)),
        "v_cache": _w((lambda *s: torch.randn(*s, dtype=torch.bfloat16) * 0.05)(B, Hkv, Sk, Dh)),
        "wq": _w((lambda *s: torch.randn(*s, dtype=torch.bfloat16) * 0.8)(D, Hq * Dh) * (D ** -0.5)),
        "wo": _w((lambda *s: torch.randn(*s, dtype=torch.bfloat16) * 0.05)(Hq * Dh, D) * ((Hq * Dh) ** -0.5)),
    }

def reference(x, k_cache, v_cache, wq, wo):
    B, Sq, D = x.shape
    Hq, Dh = 32, 128
    Hkv, Sk = k_cache.shape[1], k_cache.shape[2]
    W = min(4096, max(256, Sk // 4))
    rep = Hq // Hkv
    q = (lambda x, *d: x.permute(*d))((x @ wq).reshape(B, Sq, Hq, Dh), 0, 2, 1, 3)
    k = (lambda x, n, axis: torch.repeat_interleave(x, n, dim=axis))(k_cache[:, :, Sk - W:, :], rep, axis=1)
    v = (lambda x, n, axis: torch.repeat_interleave(x, n, dim=axis))(v_cache[:, :, Sk - W:, :], rep, axis=1)
    scores = torch.einsum("bhqd,bhkd->bhqk", q, k) * (Dh ** -0.5)
    p = _softmax(scores, axis=-1)
    o = torch.einsum("bhqk,bhkd->bhqd", p, v)
    return (lambda x, *d: x.permute(*d))(o, 0, 2, 1, 3).reshape(B, Sq, Hq * Dh) @ wo
```

```cpp
#include <hip/hip_runtime.h>
#include <hip/hip_bf16.h>
typedef unsigned short u16;
__device__ inline float b2f(u16 v){ return __uint_as_float(((unsigned)v)<<16); }
__device__ inline u16 f2b(float f){ unsigned u=__float_as_uint(f); return (u16)((u+0x7FFF+((u>>16)&1))>>16); }
__global__ void gemm_naive(const u16* A,const u16* B,u16* C,int M,int N,int K){
  int n=blockIdx.x*256+threadIdx.x, m=blockIdx.y;
  float acc=0; for(int k=0;k<K;k++) acc+=b2f(A[m*K+k])*b2f(B[(size_t)k*N+n]);
  C[m*N+n]=f2b(acc);
}
__global__ void attn_naive(const u16* q,const u16* kc,const u16* vc,u16* o){
  int s=blockIdx.x, h=blockIdx.y, b=blockIdx.z, t=threadIdx.x;
  __shared__ float qs[128]; __shared__ float p[2048]; __shared__ float red[256];
  if(t<128) qs[t]=b2f(q[(b*32+s)*4096+h*128+t]);
  __syncthreads();
  const u16* K=kc+((size_t)(b*4+h/8)*8192+6144)*128; const u16* V=vc+((size_t)(b*4+h/8)*8192+6144)*128;
  float mx=-1e30f;
  for(int j=t;j<2048;j+=256){ float a=0; for(int d=0;d<128;d++) a+=qs[d]*b2f(K[j*128+d]); a*=0.08838834764831845f; p[j]=a; mx=fmaxf(mx,a);}
  red[t]=mx; __syncthreads();
  for(int w=128;w>0;w>>=1){ if(t<w) red[t]=fmaxf(red[t],red[t+w]); __syncthreads(); }
  mx=red[0]; __syncthreads();
  float sm=0; for(int j=t;j<2048;j+=256){ float e=__expf(p[j]-mx); p[j]=e; sm+=e; }
  red[t]=sm; __syncthreads();
  for(int w=128;w>0;w>>=1){ if(t<w) red[t]+=red[t+w]; __syncthreads(); }
  sm=red[0]; __syncthreads();
  if(t<128){ float a=0; for(int j=0;j<2048;j++) a+=p[j]*b2f(V[j*128+t]); o[(b*32+s)*4096+h*128+t]=f2b(a/sm);}
}
extern "C" void kernel_launch(void* const* d_in, const int* in_sizes, int n_in, void* d_out, int out_size, void* d_ws, size_t ws_size, hipStream_t stream) {
  const u16* x=(const u16*)d_in[0]; const u16* kc=(const u16*)d_in[1]; const u16* vc=(const u16*)d_in[2]; const u16* wq=(const u16*)d_in[3]; const u16* wo=(const u16*)d_in[4];
  u16* q=(u16*)d_ws; u16* o=q+256*4096;
  gemm_naive<<<dim3(16,256),256,0,stream>>>(x,wq,q,256,4096,1024);
  attn_naive<<<dim3(32,32,8),256,0,stream>>>(q,kc,vc,o);
  gemm_naive<<<dim3(4,256),256,0,stream>>>(o,wo,(u16*)d_out,256,1024,4096);
}
```

```cpp
#include <hip/hip_runtime.h>
#include <hip/hip_bf16.h>
typedef unsigned short u16;
__device__ inline float b2f(u16 v){ return __uint_as_float(((unsigned)v)<<16); }
__device__ inline u16 f2b(float f){ unsigned u=__float_as_uint(f); return (u16)((u+0x7FFF+((u>>16)&1))>>16); }
__global__ void gemm_naive(const u16* A,const u16* B,u16* C,int M,int N,int K){
  int n=blockIdx.x*256+threadIdx.x, m=blockIdx.y;
  float acc=0; for(int k=0;k<K;k++) acc+=b2f(A[m*K+k])*b2f(B[(size_t)k*N+n]);
  C[m*N+n]=f2b(acc);
}
__global__ void attn_naive(const u16* q,const u16* kc,const u16* vc,u16* o){
  int s=blockIdx.x, h=blockIdx.y, b=blockIdx.z, t=threadIdx.x;
  __shared__ float qs[128]; __shared__ float p[2048]; __shared__ float red[256];
  if(t<128) qs[t]=b2f(q[(b*32+s)*4096+h*128+t]);
  __syncthreads();
  const u16* K=kc+((size_t)(b*4+h/8)*8192+6144)*128; const u16* V=vc+((size_t)(b*4+h/8)*8192+6144)*128;
  float mx=-1e30f;
  for(int j=t;j<2048;j+=256){ float a=0; for(int d=0;d<128;d++) a+=qs[d]*b2f(K[j*128+d]); a*=0.08838834764831845f; p[j]=a; mx=fmaxf(mx,a);}
  red[t]=mx; __syncthreads();
  for(int w=128;w>0;w>>=1){ if(t<w) red[t]=fmaxf(red[t],red[t+w]); __syncthreads(); }
  mx=red[0]; __syncthreads();
  float sm=0; for(int j=t;j<2048;j+=256){ float e=__expf(p[j]-mx); p[j]=e; sm+=e; }
  red[t]=sm; __syncthreads();
  for(int w=128;w>0;w>>=1){ if(t<w) red[t]+=red[t+w]; __syncthreads(); }
  sm=red[0]; __syncthreads();
  if(t<128){ float a=0; for(int j=0;j<2048;j++) a+=p[j]*b2f(V[j*128+t]); o[(b*32+s)*4096+h*128+t]=f2b(a/sm);}
}

typedef unsigned u32x4 __attribute__((ext_vector_type(4)));
__device__ inline unsigned long long rt(){ return __builtin_amdgcn_s_memrealtime(); }
template<int P>
__global__ __launch_bounds__(512) void probe(const char* kc,const char* vc,const char* wq,const char* wo,char* ws,char* out){
  __shared__ __attribute__((aligned(16))) char smem[131072];
  const int id=blockIdx.x; const int lane=threadIdx.x&63; const int w=__builtin_amdgcn_readfirstlane(threadIdx.x>>6);
  asm volatile(""::"s"(kc),"s"(vc),"s"(wq),"s"(wo),"s"(ws),"s"(out));
  unsigned long long t0=rt();
  if(P==0||P==1){
    const int g=id>>3,s=id&7;
    const char* kp=kc+((size_t)g*8192+6144+s*256)*256+w*8192+lane*16;
    const char* vp=vc+((size_t)g*8192+6144+s*256)*256+w*8192+lane*16;
    #pragma unroll
    for(int i=0;i<8;i++) __builtin_amdgcn_global_load_lds((const __attribute__((address_space(1))) void*)(kp+i*1024),(__attribute__((address_space(3))) void*)(smem+w*16384+i*1024),16,0,P==1?2:0);
    #pragma unroll
    for(int i=0;i<8;i++) __builtin_amdgcn_global_load_lds((const __attribute__((address_space(1))) void*)(vp+i*1024),(__attribute__((address_space(3))) void*)(smem+w*16384+8192+i*1024),16,0,P==1?2:0);
    asm volatile("s_waitcnt vmcnt(0)":::"memory");
  } else if(P==2){
    unsigned long long c0=__builtin_amdgcn_s_memtime();
    while(__builtin_amdgcn_s_memtime()-c0<24000ull){}
  } else if(P==3||P==4){
    const __amdgpu_buffer_rsrc_t rs=__builtin_amdgcn_make_buffer_rsrc(ws+(P==3?(size_t)16*1024*1024:(size_t)250*1024*1024),0,4*1024*1024,0x00020000);
    u32x4 v={1,2,3,4};
    __builtin_amdgcn_raw_buffer_store_b128(v,rs,(id*8+w)*1024+lane*16,0,16);
    asm volatile("s_waitcnt vmcnt(0)":::"memory");
  } else if(P==5||P==6){
    const char* kp=wq+(size_t)id*32768+w*4096+lane*16;
    const char* vp=wo+(size_t)id*32768+w*4096+lane*16;
    #pragma unroll
    for(int i=0;i<4;i++) __builtin_amdgcn_global_load_lds((const __attribute__((address_space(1))) void*)(kp+i*1024),(__attribute__((address_space(3))) void*)(smem+w*16384+i*1024),16,0,P==6?2:0);
    #pragma unroll
    for(int i=0;i<4;i++) __builtin_amdgcn_global_load_lds((const __attribute__((address_space(1))) void*)(vp+i*1024),(__attribute__((address_space(3))) void*)(smem+w*16384+8192+i*1024),16,0,P==6?2:0);
    asm volatile("s_waitcnt vmcnt(0)":::"memory");
  }
  unsigned long long t1=rt();
  unsigned long long d=t1-t0; if(d>5000) d=5000;
  while(rt()-t0<d*200){ __builtin_amdgcn_s_sleep(8); }
  if(ws==(char*)1) ws[threadIdx.x]=smem[threadIdx.x*77];
}
extern "C" void kernel_launch(void* const* d_in, const int* in_sizes, int n_in, void* d_out, int out_size, void* d_ws, size_t ws_size, hipStream_t stream) {
  const u16* x=(const u16*)d_in[0]; const u16* kc=(const u16*)d_in[1]; const u16* vc=(const u16*)d_in[2]; const u16* wq=(const u16*)d_in[3]; const u16* wo=(const u16*)d_in[4];
  u16* q=(u16*)d_ws; u16* o=q+256*4096;
  probe<5><<<256,512,0,stream>>>((const char*)kc,(const char*)vc,(const char*)wq,(const char*)wo,(char*)d_ws,(char*)d_out);
  gemm_naive<<<dim3(16,256),256,0,stream>>>(x,wq,q,256,4096,1024);
  attn_naive<<<dim3(32,32,8),256,0,stream>>>(q,kc,vc,o);
  gemm_naive<<<dim3(4,256),256,0,stream>>>(o,wo,(u16*)d_out,256,1024,4096);
}
```

```cpp
#include <hip/hip_runtime.h>

__global__ __launch_bounds__(512, 2) void fused_attn_block(const void* x, const void* kc, const void* vc, const void* wq,
                                                           const void* wo, void* out, void* ws) {
  __shared__ __attribute__((aligned(16))) char lds[132608];
  unsigned tid = threadIdx.x, bid = blockIdx.x;
  unsigned ldsbase = (unsigned)(size_t)(__attribute__((address_space(3))) char*)lds;
  asm volatile(
    "s_load_dwordx8 s[4:11], s[0:1], 0x0\n"
    "s_load_dwordx4 s[12:15], s[0:1], 0x20\n"
    "s_load_dwordx2 s[16:17], s[0:1], 0x30\n"
    "v_readfirstlane_b32 s18, v0\n"
    "v_and_b32 v1, 63, v0\n"
    "s_and_b32 s19, s2, 7\n"
    "s_lshr_b32 s20, s2, 3\n"
    "s_lshr_b32 s18, s18, 6\n"
    "s_lshr_b32 s24, s20, 3\n"
    "s_and_b32 s25, s20, 7\n"
    "s_and_b32 s33, s18, 3\n"
    "s_and_b32 s21, s19, 1\n"
    "s_lshl_b32 s21, s21, 2\n"
    "s_add_u32 s21, s21, s24\n"
    "s_and_b32 s22, s19, 6\n"
    "s_bfe_u32 s31, s20, 0x10002\n"
    "s_add_u32 s22, s22, s31\n"
    "s_and_b32 s23, s20, 3\n"
    "v_lshlrev_b32 v2, 4, v1\n"
    "s_mov_b32 s62, 4194304\n"
    "s_cmp_ge_u32 s18, 4\n"
    "s_cbranch_scc1 .Lfa_B\n"
    "s_setprio 2\n"
    "s_lshr_b32 s44, s18, 1\n"
    "s_lshl_b32 s47, s24, 1\n"
    "s_add_u32 s47, s47, s44\n"
    "s_lshl_b32 s47, s47, 7\n"
    "s_lshl_b32 s30, s19, 4\n"
    "s_add_u32 s47, s47, s30\n"
    "s_lshl_b32 s30, s25, 1\n"
    "s_add_u32 s47, s47, s30\n"
    "s_and_b32 s30, s18, 1\n"
    "s_add_u32 s47, s47, s30\n"
    "s_lshl_b32 s46, s47, 11\n"
    "s_add_u32 s46, s46, 1048576\n"
    "s_lshl_b32 s47, s47, 7\n"
    "s_add_u32 s47, s47, 65536\n"
    "s_lshl_b32 s48, s21, 3\n"
    "s_add_u32 s48, s48, s22\n"
    "s_lshl_b32 s48, s48, 2\n"
    "s_add_u32 s48, s48, s18\n"
    "s_lshl_b32 s53, s48, 9\n"
    "s_add_u32 s53, s53, 65536\n"
    "s_waitcnt lgkmcnt(0)\n"
    "s_load_dwordx2 s[26:27], s[16:17], 0\n"
    "s_mov_b32 s36, s16\n"
    "s_and_b32 s37, s17, 0xffff\n"
    "s_mov_b32 s38, 25165824\n"
    "s_mov_b32 s39, 0x20000\n"
    "v_lshlrev_b32 v234, 2, v1\n"
    "s_lshl_b32 s30, s18, 8\n"
    "s_add_u32 s30, s30, 131584\n"
    "v_add_u32 v12, s30, v234\n"
    "v_add_u32 v10, 131072, v234\n"
    "v_mov_b32 v11, 0\n"
    "v_mov_b32 v9, 0\n"
    "ds_write_b32 v12, v11\n"
    "ds_write_b32 v10, v11\n"
    "v_and_b32 v234, 7, v1\n"
    "v_lshlrev_b32 v234, 6, v234\n"
    "v_lshrrev_b32 v235, 5, v1\n"
    "v_bfe_u32 v236, v1, 2, 2\n"
    "v_xor_b32 v237, v235, v236\n"
    "v_lshl_add_u32 v234, v237, 4, v234\n"
    "v_bfe_u32 v237, v1, 3, 2\n"
    "s_lshl_b32 s30, s44, 12\n"
    "s_add_u32 s30, s30, 32768\n"
    "v_lshl_add_u32 v3, v237, 10, v234\n"
    "v_add_u32 v3, s30, v3\n"
    "v_xor_b32 v4, 32, v3\n"
    "v_lshl_add_u32 v242, v237, 11, v234\n"
    "v_bfe_u32 v237, v1, 4, 1\n"
    "v_lshlrev_b32 v237, 5, v237\n"
    "v_lshl_add_u32 v237, v236, 6, v237\n"
    "v_and_b32 v238, 3, v1\n"
    "v_lshl_add_u32 v243, v238, 3, v237\n"
    "v_lshl_add_u32 v243, v235, 8, v243\n"
    "v_add_u32 v243, 65536, v243\n"
    "v_and_b32 v239, 1, v1\n"
    "v_bfe_u32 v240, v1, 1, 1\n"
    "v_lshl_add_u32 v239, v239, 1, v240\n"
    "v_lshl_add_u32 v5, v239, 3, v237\n"
    "v_lshl_add_u32 v5, v235, 10, v5\n"
    "s_and_b32 s30, s18, 1\n"
    "s_lshl_b32 s30, s30, 9\n"
    "s_add_u32 s30, s30, 81920\n"
    "v_add_u32 v5, s30, v5\n"
    "v_lshlrev_b32 v244, 7, v1\n"
    "v_mov_b32 v13, 0\n"
    "v_mov_b32 v14, 0\n"
    "s_mov_b32 s52, 1092616192\n"
    "v_mov_b32 v48, 0\n"
    "v_mov_b32 v49, 0\n"
    "v_mov_b32 v50, 0\n"
    "v_mov_b32 v51, 0\n"
    "v_mov_b32 v52, 0\n"
    "v_mov_b32 v53, 0\n"
    "v_mov_b32 v54, 0\n"
    "v_mov_b32 v55, 0\n"
    "v_mov_b32 v56, 0\n"
    "v_mov_b32 v57, 0\n"
    "v_mov_b32 v58, 0\n"
    "v_mov_b32 v59, 0\n"
    "v_mov_b32 v60, 0\n"
    "v_mov_b32 v61, 0\n"
    "v_mov_b32 v62, 0\n"
    "v_mov_b32 v63, 0\n"
    "v_mov_b32 v64, 0\n"
    "v_mov_b32 v65, 0\n"
    "v_mov_b32 v66, 0\n"
    "v_mov_b32 v67, 0\n"
    "v_mov_b32 v68, 0\n"
    "v_mov_b32 v69, 0\n"
    "v_mov_b32 v70, 0\n"
    "v_mov_b32 v71, 0\n"
    "v_mov_b32 v72, 0\n"
    "v_mov_b32 v73, 0\n"
    "v_mov_b32 v74, 0\n"
    "v_mov_b32 v75, 0\n"
    "v_mov_b32 v76, 0\n"
    "v_mov_b32 v77, 0\n"
    "v_mov_b32 v78, 0\n"
    "v_mov_b32 v79, 0\n"
    "v_mov_b32 v80, 0\n"
    "v_mov_b32 v81, 0\n"
    "v_mov_b32 v82, 0\n"
    "v_mov_b32 v83, 0\n"
    "v_mov_b32 v84, 0\n"
    "v_mov_b32 v85, 0\n"
    "v_mov_b32 v86, 0\n"
    "v_mov_b32 v87, 0\n"
    "v_mov_b32 v88, 0\n"
    "v_mov_b32 v89, 0\n"
    "v_mov_b32 v90, 0\n"
    "v_mov_b32 v91, 0\n"
    "v_mov_b32 v92, 0\n"
    "v_mov_b32 v93, 0\n"
    "v_mov_b32 v94, 0\n"
    "v_mov_b32 v95, 0\n"
    "v_mov_b32 v96, 0\n"
    "v_mov_b32 v97, 0\n"
    "v_mov_b32 v98, 0\n"
    "v_mov_b32 v99, 0\n"
    "v_mov_b32 v100, 0\n"
    "v_mov_b32 v101, 0\n"
    "v_mov_b32 v102, 0\n"
    "v_mov_b32 v103, 0\n"
    "v_mov_b32 v104, 0\n"
    "v_mov_b32 v105, 0\n"
    "v_mov_b32 v106, 0\n"
    "v_mov_b32 v107, 0\n"
    "v_mov_b32 v108, 0\n"
    "v_mov_b32 v109, 0\n"
    "v_mov_b32 v110, 0\n"
    "v_mov_b32 v111, 0\n"
    "v_mov_b32 v112, 0\n"
    "v_mov_b32 v113, 0\n"
    "v_mov_b32 v114, 0\n"
    "v_mov_b32 v115, 0\n"
    "v_mov_b32 v116, 0\n"
    "v_mov_b32 v117, 0\n"
    "v_mov_b32 v118, 0\n"
    "v_mov_b32 v119, 0\n"
    "v_mov_b32 v120, 0\n"
    "v_mov_b32 v121, 0\n"
    "v_mov_b32 v122, 0\n"
    "v_mov_b32 v123, 0\n"
    "v_mov_b32 v124, 0\n"
    "v_mov_b32 v125, 0\n"
    "v_mov_b32 v126, 0\n"
    "v_mov_b32 v127, 0\n"
    "s_mov_b32 s44, 0\n"
    "s_mov_b32 s45, 0\n"
    "s_waitcnt lgkmcnt(0)\n"
    "s_mul_hi_u32 s30, s26, 1284865837\n"
    "s_mul_i32 s31, s26, 1481765933\n"
    "s_add_u32 s30, s30, s31\n"
    "s_mul_i32 s31, s27, 1284865837\n"
    "s_add_u32 s30, s30, s31\n"
    "s_mul_i32 s26, s26, 1284865837\n"
    "s_add_u32 s26, s26, 4150755663\n"
    "s_addc_u32 s27, s30, 335903614\n"
    ".Lfa_qloop:\n"
    "v_add_u32 v6, s44, v3\n"
    "v_add_u32 v7, s44, v4\n"
    "v_add_u32 v8, s44, v5\n"
    "s_add_u32 s44, s44, 16384\n"
    "s_cmp_ge_u32 s44, 49152\n"
    "s_cselect_b32 s30, 49152, 0\n"
    "s_sub_u32 s44, s44, s30\n"
    "s_add_u32 s45, s45, 1\n"
    "s_cmp_lt_u32 s45, 8\n"
    "s_barrier\n"
    "ds_read_b128 v[160:163], v6 offset:0\n"
    "ds_read_b64_tr_b16 v[192:193], v8 offset:0\n"
    "ds_read_b64_tr_b16 v[194:195], v8 offset:256\n"
    "ds_read_b128 v[164:167], v7 offset:0\n"
    "ds_read_b64_tr_b16 v[196:197], v8 offset:2048\n"
    "ds_read_b64_tr_b16 v[198:199], v8 offset:2304\n"
    "ds_read_b128 v[168:171], v6 offset:512\n"
    "ds_read_b64_tr_b16 v[200:201], v8 offset:4096\n"
    "ds_read_b64_tr_b16 v[202:203], v8 offset:4352\n"
    "ds_read_b128 v[172:175], v7 offset:512\n"
    "ds_read_b64_tr_b16 v[204:205], v8 offset:6144\n"
    "ds_read_b64_tr_b16 v[206:207], v8 offset:6400\n"
    "ds_read_b128 v[176:179], v6 offset:8192\n"
    "ds_read_b64_tr_b16 v[208:209], v8 offset:8192\n"
    "ds_read_b64_tr_b16 v[210:211], v8 offset:8448\n"
    "s_waitcnt lgkmcnt(12)\n"
    "v_mfma_f32_32x32x16_bf16 v[112:127], v[192:195], v[160:163], v[112:127]\n"
    "ds_read_b128 v[180:183], v7 offset:8192\n"
    "ds_read_b64_tr_b16 v[212:213], v8 offset:10240\n"
    "ds_read_b64_tr_b16 v[214:215], v8 offset:10496\n"
    "s_waitcnt lgkmcnt(12)\n"
    "v_mfma_f32_32x32x16_bf16 v[112:127], v[196:199], v[164:167], v[112:127]\n"
    "ds_read_b128 v[184:187], v6 offset:8704\n"
    "ds_read_b64_tr_b16 v[216:217], v8 offset:12288\n"
    "ds_read_b64_tr_b16 v[218:219], v8 offset:12544\n"
    "s_waitcnt lgkmcnt(12)\n"
    "v_mfma_f32_32x32x16_bf16 v[112:127], v[200:203], v[168:171], v[112:127]\n"
    "ds_read_b128 v[188:191], v7 offset:8704\n"
    "ds_read_b64_tr_b16 v[220:221], v8 offset:14336\n"
    "ds_read_b64_tr_b16 v[222:223], v8 offset:14592\n"
    "s_waitcnt lgkmcnt(12)\n"
    "v_mfma_f32_32x32x16_bf16 v[112:127], v[204:207], v[172:175], v[112:127]\n"
    "s_waitcnt lgkmcnt(9)\n"
    "v_mfma_f32_32x32x16_bf16 v[112:127], v[208:211], v[176:179], v[112:127]\n"
    "s_waitcnt lgkmcnt(6)\n"
    "v_mfma_f32_32x32x16_bf16 v[112:127], v[212:215], v[180:183], v[112:127]\n"
    "s_waitcnt lgkmcnt(3)\n"
    "v_mfma_f32_32x32x16_bf16 v[112:127], v[216:219], v[184:187], v[112:127]\n"
    "s_waitcnt lgkmcnt(0)\n"
    "v_mfma_f32_32x32x16_bf16 v[112:127], v[220:223], v[188:191], v[112:127]\n"
    "s_cbranch_scc1 .Lfa_qloop\n"
    "s_mov_b32 s30, 1040356334\n"
    "s_mov_b32 s31, 1040356334\n"
    "v_mov_b32 v6, v242\n"
    "v_xor_b32 v7, 32, v242\n"
    "v_xor_b32 v4, 32, v242\n"
    "v_mov_b32 v8, v243\n"
    "v_mov_b32 v11, 1\n"
    "s_nop 3\n"
    "v_pk_mul_f32 v[112:113], v[112:113], s[30:31]\n"
    "v_pk_mul_f32 v[114:115], v[114:115], s[30:31]\n"
    "v_pk_mul_f32 v[116:117], v[116:117], s[30:31]\n"
    "v_pk_mul_f32 v[118:119], v[118:119], s[30:31]\n"
    "v_cvt_pk_bf16_f32 v224, v112, v113\n"
    "v_cvt_pk_bf16_f32 v225, v114, v115\n"
    "v_cvt_pk_bf16_f32 v226, v116, v117\n"
    "v_cvt_pk_bf16_f32 v227, v118, v119\n"
    "buffer_store_dwordx4 v[224:227], v2, s[36:39], s46 offen offset:0 sc1\n"
    "v_pk_mul_f32 v[120:121], v[120:121], s[30:31]\n"
    "v_pk_mul_f32 v[122:123], v[122:123], s[30:31]\n"
    "v_pk_mul_f32 v[124:125], v[124:125], s[30:31]\n"
    "v_pk_mul_f32 v[126:127], v[126:127], s[30:31]\n"
    "v_cvt_pk_bf16_f32 v228, v120, v121\n"
    "v_cvt_pk_bf16_f32 v229, v122, v123\n"
    "v_cvt_pk_bf16_f32 v230, v124, v125\n"
    "v_cvt_pk_bf16_f32 v231, v126, v127\n"
    "buffer_store_dwordx4 v[228:231], v2, s[36:39], s46 offen offset:1024 sc1\n"
    "v_mov_b32 v234, s26\n"
    "v_mov_b32 v235, s27\n"
    "v_mov_b32 v236, s26\n"
    "v_mov_b32 v237, s27\n"
    "s_waitcnt vmcnt(0)\n"
    "s_mov_b64 exec, 0xff\n"
    "buffer_store_dwordx4 v[234:237], v2, s[36:39], s47 offen sc1\n"
    "s_mov_b64 exec, -1\n"
    "s_lshl_b32 s46, s48, 13\n"
    "s_add_u32 s46, s46, 1048576\n"
    "s_add_u32 s47, s46, 4096\n"
    "s_mov_b32 s44, 16384\n"
    "s_mov_b32 s45, 2\n"
    "s_mov_b32 s49, 65536\n"
    "s_mov_b64 exec, 0xf\n"
    ".Lfa_fp_1:\n"
    "buffer_load_dwordx2 v[238:239], v244, s[36:39], s53 offen sc1\n"
    "s_sub_u32 s49, s49, 1\n"
    "s_cbranch_scc1 .Lfa_fpd_2\n"
    "s_waitcnt vmcnt(0)\n"
    "v_cmp_ne_u64 vcc, s[26:27], v[238:239]\n"
    "s_cbranch_vccnz .Lfa_fp_1\n"
    ".Lfa_fpd_2:\n"
    "s_mov_b64 exec, -1\n"
    "buffer_load_dwordx4 v[16:19], v2, s[36:39], s46 offen offset:0 sc1\n"
    "buffer_load_dwordx4 v[20:23], v2, s[36:39], s46 offen offset:1024 sc1\n"
    "buffer_load_dwordx4 v[24:27], v2, s[36:39], s46 offen offset:2048 sc1\n"
    "buffer_load_dwordx4 v[28:31], v2, s[36:39], s46 offen offset:3072 sc1\n"
    "buffer_load_dwordx4 v[32:35], v2, s[36:39], s47 offen offset:0 sc1\n"
    "buffer_load_dwordx4 v[36:39], v2, s[36:39], s47 offen offset:1024 sc1\n"
    "buffer_load_dwordx4 v[40:43], v2, s[36:39], s47 offen offset:2048 sc1\n"
    "buffer_load_dwordx4 v[44:47], v2, s[36:39], s47 offen offset:3072 sc1\n"
    "ds_write_b32 v12, v11\n"
    ".Lfa_lw_3:\n"
    "ds_read_b32 v9, v10\n"
    "s_sub_u32 s62, s62, 1\n"
    "s_cselect_b32 s62, 0, s62\n"
    "s_waitcnt lgkmcnt(0)\n"
    "s_cbranch_scc1 .Lfa_lwd_4\n"
    "v_cmp_gt_u32 vcc, 1, v9\n"
    "s_cbranch_vccnz .Lfa_lw_3\n"
    ".Lfa_lwd_4:\n"
    "ds_read_b128 v[160:163], v6 offset:0\n"
    "ds_read_b128 v[164:167], v7 offset:0\n"
    "ds_read_b128 v[168:171], v6 offset:512\n"
    "ds_read_b128 v[172:175], v7 offset:512\n"
    "ds_read_b128 v[176:179], v6 offset:1024\n"
    "ds_read_b128 v[180:183], v7 offset:1024\n"
    "ds_read_b128 v[184:187], v6 offset:1536\n"
    "ds_read_b128 v[188:191], v7 offset:1536\n"
    "v_mov_b32 v11, 2\n"
    "s_waitcnt lgkmcnt(0)\n"
    "s_waitcnt vmcnt(7)\n"
    "v_mfma_f32_32x32x16_bf16 v[112:127], v[160:163], v[16:19], 0\n"
    "s_waitcnt vmcnt(6)\n"
    "v_mfma_f32_32x32x16_bf16 v[112:127], v[164:167], v[20:23], v[112:127]\n"
    "ds_read_b128 v[160:163], v6 offset:8192\n"
    "s_waitcnt vmcnt(5)\n"
    "v_mfma_f32_32x32x16_bf16 v[112:127], v[168:171], v[24:27], v[112:127]\n"
    "ds_read_b128 v[164:167], v7 offset:8192\n"
    "s_waitcnt vmcnt(4)\n"
    "v_mfma_f32_32x32x16_bf16 v[112:127], v[172:175], v[28:31], v[112:127]\n"
    "ds_read_b128 v[168:171], v6 offset:8704\n"
    "s_waitcnt vmcnt(3)\n"
    "v_mfma_f32_32x32x16_bf16 v[112:127], v[176:179], v[32:35], v[112:127]\n"
    "ds_read_b128 v[172:175], v7 offset:8704\n"
    "s_waitcnt vmcnt(2)\n"
    "v_mfma_f32_32x32x16_bf16 v[112:127], v[180:183], v[36:39], v[112:127]\n"
    "ds_read_b128 v[176:179], v6 offset:9216\n"
    "s_waitcnt vmcnt(1)\n"
    "v_mfma_f32_32x32x16_bf16 v[112:127], v[184:187], v[40:43], v[112:127]\n"
    "ds_read_b128 v[180:183], v7 offset:9216\n"
    "s_waitcnt vmcnt(0)\n"
    "v_mfma_f32_32x32x16_bf16 v[112:127], v[188:191], v[44:47], v[112:127]\n"
    "ds_read_b128 v[184:187], v6 offset:9728\n"
    "s_nop 3\n"
    "ds_read_b128 v[188:191], v7 offset:9728\n"
    "ds_write_b32 v12, v11\n"
    "ds_read_b32 v9, v10\n"
    "v_add_u32 v6, s44, v242\n"
    "v_add_u32 v7, s44, v4\n"
    "s_nop 2\n"
    "v_max3_f32 v232, v112, v113, v114\n"
    "v_max3_f32 v233, v115, v116, v117\n"
    "v_max3_f32 v232, v232, v118, v119\n"
    "v_max3_f32 v233, v233, v120, v121\n"
    "v_max3_f32 v232, v232, v122, v123\n"
    "v_max3_f32 v233, v233, v124, v125\n"
    "v_max3_f32 v232, v232, v126, v127\n"
    "v_max_f32 v232, v232, v233\n"
    "v_mov_b32 v233, v232\n"
    "s_nop 1\n"
    "v_permlane32_swap_b32 v232, v233\n"
    "v_max_f32 v232, v232, v233\n"
    "v_ceil_f32 v232, v232\n"
    "v_mov_b32 v15, v232\n"
    "v_sub_f32 v144, 0, v232\n"
    "v_sub_f32 v145, 0, v232\n"
    "v_sub_f32 v146, 0, v232\n"
    "v_sub_f32 v147, 0, v232\n"
    "v_sub_f32 v148, 0, v232\n"
    "v_sub_f32 v149, 0, v232\n"
    "v_sub_f32 v150, 0, v232\n"
    "v_sub_f32 v151, 0, v232\n"
    "v_sub_f32 v152, 0, v232\n"
    "v_sub_f32 v153, 0, v232\n"
    "v_sub_f32 v154, 0, v232\n"
    "v_sub_f32 v155, 0, v232\n"
    "v_sub_f32 v156, 0, v232\n"
    "v_sub_f32 v157, 0, v232\n"
    "v_sub_f32 v158, 0, v232\n"
    "v_sub_f32 v159, 0, v232\n"
    "v_sub_f32 v112, v112, v232\n"
    "v_sub_f32 v113, v113, v232\n"
    "v_sub_f32 v114, v114, v232\n"
    "v_sub_f32 v115, v115, v232\n"
    "v_sub_f32 v116, v116, v232\n"
    "v_sub_f32 v117, v117, v232\n"
    "v_sub_f32 v118, v118, v232\n"
    "v_sub_f32 v119, v119, v232\n"
    "v_sub_f32 v120, v120, v232\n"
    "v_sub_f32 v121, v121, v232\n"
    "v_sub_f32 v122, v122, v232\n"
    "v_sub_f32 v123, v123, v232\n"
    "v_sub_f32 v124, v124, v232\n"
    "v_sub_f32 v125, v125, v232\n"
    "v_sub_f32 v126, v126, v232\n"
    "v_sub_f32 v127, v127, v232\n"
    "s_waitcnt lgkmcnt(0)\n"
    "v_cmp_gt_u32 vcc, s45, v9\n"
    "s_cbranch_vccz .Lfa_lwd_6\n"
    ".Lfa_lw_5:\n"
    "ds_read_b32 v9, v10\n"
    "s_sub_u32 s62, s62, 1\n"
    "s_cselect_b32 s62, 0, s62\n"
    "s_waitcnt lgkmcnt(0)\n"
    "s_cbranch_scc1 .Lfa_lwd_6\n"
    "v_cmp_gt_u32 vcc, s45, v9\n"
    "s_cbranch_vccnz .Lfa_lw_5\n"
    ".Lfa_lwd_6:\n"
    ".Lfa_aloop:\n"
    "v_mfma_f32_32x32x16_bf16 v[128:143], v[160:163], v[16:19], v[144:159]\n"
    "ds_read_b64_tr_b16 v[192:193], v8 offset:0\n"
    "ds_read_b64_tr_b16 v[194:195], v8 offset:2048\n"
    "v_exp_f32 v112, v112\n"
    "v_exp_f32 v113, v113\n"
    "v_mfma_f32_32x32x16_bf16 v[128:143], v[164:167], v[20:23], v[128:143]\n"
    "ds_read_b64_tr_b16 v[196:197], v8 offset:512\n"
    "ds_read_b64_tr_b16 v[198:199], v8 offset:2560\n"
    "v_exp_f32 v114, v114\n"
    "v_exp_f32 v115, v115\n"
    "v_mfma_f32_32x32x16_bf16 v[128:143], v[168:171], v[24:27], v[128:143]\n"
    "ds_read_b64_tr_b16 v[200:201], v8 offset:1024\n"
    "ds_read_b64_tr_b16 v[202:203], v8 offset:3072\n"
    "v_exp_f32 v116, v116\n"
    "v_cvt_pk_bf16_f32 v224, v112, v113\n"
    "v_cvt_pk_bf16_f32 v225, v114, v115\n"
    "v_mfma_f32_32x32x16_bf16 v[128:143], v[172:175], v[28:31], v[128:143]\n"
    "ds_read_b64_tr_b16 v[204:205], v8 offset:1536\n"
    "ds_read_b64_tr_b16 v[206:207], v8 offset:3584\n"
    "v_exp_f32 v117, v117\n"
    "v_exp_f32 v118, v118\n"
    "v_add_u32 v11, 1, v11\n"
    "v_mfma_f32_32x32x16_bf16 v[128:143], v[176:179], v[32:35], v[128:143]\n"
    "ds_read_b64_tr_b16 v[208:209], v8 offset:4096\n"
    "ds_read_b64_tr_b16 v[210:211], v8 offset:6144\n"
    "v_exp_f32 v119, v119\n"
    "v_cvt_pk_bf16_f32 v226, v116, v117\n"
    "v_add_f32 v13, v13, v112\n"
    "v_mfma_f32_32x32x16_bf16 v[128:143], v[180:183], v[36:39], v[128:143]\n"
    "ds_read_b64_tr_b16 v[212:213], v8 offset:4608\n"
    "ds_read_b64_tr_b16 v[214:215], v8 offset:6656\n"
    "v_exp_f32 v120, v120\n"
    "v_cvt_pk_bf16_f32 v227, v118, v119\n"
    "v_add_f32 v14, v14, v113\n"
    "v_mfma_f32_32x32x16_bf16 v[128:143], v[184:187], v[40:43], v[128:143]\n"
    "ds_read_b64_tr_b16 v[216:217], v8 offset:5120\n"
    "ds_read_b64_tr_b16 v[218:219], v8 offset:7168\n"
    "v_exp_f32 v121, v121\n"
    "v_exp_f32 v122, v122\n"
    "v_mfma_f32_32x32x16_bf16 v[128:143], v[188:191], v[44:47], v[128:143]\n"
    "ds_read_b64_tr_b16 v[220:221], v8 offset:5632\n"
    "ds_read_b64_tr_b16 v[222:223], v8 offset:7680\n"
    "v_exp_f32 v123, v123\n"
    "v_cvt_pk_bf16_f32 v228, v120, v121\n"
    "s_waitcnt lgkmcnt(8)\n"
    "v_mfma_f32_32x32x16_bf16 v[48:63], v[192:195], v[224:227], v[48:63]\n"
    "ds_read_b128 v[160:163], v6 offset:0\n"
    "v_exp_f32 v124, v124\n"
    "v_exp_f32 v125, v125\n"
    "v_cvt_pk_bf16_f32 v229, v122, v123\n"
    "v_mfma_f32_32x32x16_bf16 v[64:79], v[196:199], v[224:227], v[64:79]\n"
    "ds_read_b128 v[164:167], v7 offset:0\n"
    "v_exp_f32 v126, v126\n"
    "v_exp_f32 v127, v127\n"
    "v_cvt_pk_bf16_f32 v230, v124, v125\n"
    "v_mfma_f32_32x32x16_bf16 v[80:95], v[200:203], v[224:227], v[80:95]\n"
    "ds_read_b128 v[168:171], v6 offset:512\n"
    "ds_read_b128 v[172:175], v7 offset:512\n"
    "v_add_f32 v13, v13, v114\n"
    "v_cvt_pk_bf16_f32 v231, v126, v127\n"
    "v_add_f32 v14, v14, v115\n"
    "v_add_f32 v13, v13, v116\n"
    "v_mfma_f32_32x32x16_bf16 v[96:111], v[204:207], v[224:227], v[96:111]\n"
    "ds_read_b128 v[176:179], v6 offset:1024\n"
    "v_add_f32 v14, v14, v117\n"
    "v_max3_f32 v232, v128, v129, v130\n"
    "v_add_f32 v13, v13, v118\n"
    "v_max3_f32 v233, v131, v132, v133\n"
    "s_waitcnt lgkmcnt(5)\n"
    "v_mfma_f32_32x32x16_bf16 v[48:63], v[208:211], v[228:231], v[48:63]\n"
    "ds_read_b128 v[180:183], v7 offset:1024\n"
    "ds_read_b128 v[184:187], v6 offset:1536\n"
    "v_add_f32 v14, v14, v119\n"
    "v_max3_f32 v232, v232, v134, v135\n"
    "v_add_f32 v13, v13, v120\n"
    "v_max3_f32 v233, v233, v136, v137\n"
    "v_mfma_f32_32x32x16_bf16 v[64:79], v[212:215], v[228:231], v[64:79]\n"
    "ds_read_b128 v[188:191], v7 offset:1536\n"
    "v_add_f32 v14, v14, v121\n"
    "v_max3_f32 v232, v232, v138, v139\n"
    "v_add_f32 v13, v13, v122\n"
    "v_max3_f32 v233, v233, v140, v141\n"
    "v_max3_f32 v232, v232, v142, v143\n"
    "v_mfma_f32_32x32x16_bf16 v[80:95], v[216:219], v[228:231], v[80:95]\n"
    "v_max_f32 v232, v232, v233\n"
    "v_add_f32 v14, v14, v123\n"
    "v_cmp_lt_f32 vcc, s52, v232\n"
    "v_add_f32 v13, v13, v124\n"
    "v_add_f32 v14, v14, v125\n"
    "v_mfma_f32_32x32x16_bf16 v[96:111], v[220:223], v[228:231], v[96:111]\n"
    "v_add_f32 v13, v13, v126\n"
    "v_add_f32 v14, v14, v127\n"
    "s_cmp_eq_u32 s45, 9\n"
    "s_waitcnt lgkmcnt(0)\n"
    "s_cbranch_vccnz .Lfa_rareA\n"
    ".Lfa_backA:\n"
    "s_cbranch_scc1 .Lfa_atail\n"
    "v_mfma_f32_32x32x16_bf16 v[112:127], v[160:163], v[16:19], v[144:159]\n"
    "ds_read_b64_tr_b16 v[192:193], v8 offset:8192\n"
    "ds_read_b64_tr_b16 v[194:195], v8 offset:10240\n"
    "v_exp_f32 v128, v128\n"
    "v_exp_f32 v129, v129\n"
    "v_mfma_f32_32x32x16_bf16 v[112:127], v[164:167], v[20:23], v[112:127]\n"
    "ds_read_b64_tr_b16 v[196:197], v8 offset:8704\n"
    "ds_read_b64_tr_b16 v[198:199], v8 offset:10752\n"
    "v_exp_f32 v130, v130\n"
    "v_exp_f32 v131, v131\n"
    "v_mfma_f32_32x32x16_bf16 v[112:127], v[168:171], v[24:27], v[112:127]\n"
    "ds_read_b64_tr_b16 v[200:201], v8 offset:9216\n"
    "ds_read_b64_tr_b16 v[202:203], v8 offset:11264\n"
    "v_exp_f32 v132, v132\n"
    "v_cvt_pk_bf16_f32 v224, v128, v129\n"
    "v_cvt_pk_bf16_f32 v225, v130, v131\n"
    "v_mfma_f32_32x32x16_bf16 v[112:127], v[172:175], v[28:31], v[112:127]\n"
    "ds_read_b64_tr_b16 v[204:205], v8 offset:9728\n"
    "ds_read_b64_tr_b16 v[206:207], v8 offset:11776\n"
    "v_exp_f32 v133, v133\n"
    "v_exp_f32 v134, v134\n"
    "v_mfma_f32_32x32x16_bf16 v[112:127], v[176:179], v[32:35], v[112:127]\n"
    "ds_read_b64_tr_b16 v[208:209], v8 offset:12288\n"
    "ds_read_b64_tr_b16 v[210:211], v8 offset:14336\n"
    "v_exp_f32 v135, v135\n"
    "v_cvt_pk_bf16_f32 v226, v132, v133\n"
    "v_add_f32 v13, v13, v128\n"
    "v_mfma_f32_32x32x16_bf16 v[112:127], v[180:183], v[36:39], v[112:127]\n"
    "ds_read_b64_tr_b16 v[212:213], v8 offset:12800\n"
    "ds_read_b64_tr_b16 v[214:215], v8 offset:14848\n"
    "v_exp_f32 v136, v136\n"
    "v_cvt_pk_bf16_f32 v227, v134, v135\n"
    "v_add_f32 v14, v14, v129\n"
    "v_mfma_f32_32x32x16_bf16 v[112:127], v[184:187], v[40:43], v[112:127]\n"
    "ds_read_b64_tr_b16 v[216:217], v8 offset:13312\n"
    "ds_read_b64_tr_b16 v[218:219], v8 offset:15360\n"
    "v_exp_f32 v137, v137\n"
    "v_exp_f32 v138, v138\n"
    "v_mfma_f32_32x32x16_bf16 v[112:127], v[188:191], v[44:47], v[112:127]\n"
    "ds_read_b64_tr_b16 v[220:221], v8 offset:13824\n"
    "ds_read_b64_tr_b16 v[222:223], v8 offset:15872\n"
    "v_exp_f32 v139, v139\n"
    "v_cvt_pk_bf16_f32 v228, v136, v137\n"
    "s_waitcnt lgkmcnt(8)\n"
    "v_mfma_f32_32x32x16_bf16 v[48:63], v[192:195], v[224:227], v[48:63]\n"
    "ds_read_b128 v[160:163], v6 offset:8192\n"
    "ds_read_b32 v9, v10\n"
    "s_add_u32 s45, s45, 1\n"
    "v_exp_f32 v140, v140\n"
    "v_exp_f32 v141, v141\n"
    "v_cvt_pk_bf16_f32 v229, v138, v139\n"
    "v_mfma_f32_32x32x16_bf16 v[64:79], v[196:199], v[224:227], v[64:79]\n"
    "ds_read_b128 v[164:167], v7 offset:8192\n"
    "v_exp_f32 v142, v142\n"
    "v_exp_f32 v143, v143\n"
    "v_cvt_pk_bf16_f32 v230, v140, v141\n"
    "v_add_u32 v8, s44, v243\n"
    "v_mfma_f32_32x32x16_bf16 v[80:95], v[200:203], v[224:227], v[80:95]\n"
    "ds_read_b128 v[168:171], v6 offset:8704\n"
    "ds_read_b128 v[172:175], v7 offset:8704\n"
    "v_add_f32 v13, v13, v130\n"
    "v_cvt_pk_bf16_f32 v231, v142, v143\n"
    "v_add_f32 v14, v14, v131\n"
    "v_add_f32 v13, v13, v132\n"
    "s_add_u32 s44, s44, 16384\n"
    "s_and_b32 s44, s44, 65535\n"
    "v_mfma_f32_32x32x16_bf16 v[96:111], v[204:207], v[224:227], v[96:111]\n"
    "ds_read_b128 v[176:179], v6 offset:9216\n"
    "v_add_f32 v14, v14, v133\n"
    "v_max3_f32 v232, v112, v113, v114\n"
    "v_add_f32 v13, v13, v134\n"
    "v_max3_f32 v233, v115, v116, v117\n"
    "s_waitcnt lgkmcnt(4)\n"
    "v_mfma_f32_32x32x16_bf16 v[48:63], v[208:211], v[228:231], v[48:63]\n"
    "ds_read_b128 v[180:183], v7 offset:9216\n"
    "ds_read_b128 v[184:187], v6 offset:9728\n"
    "v_add_f32 v14, v14, v135\n"
    "v_max3_f32 v232, v232, v118, v119\n"
    "v_add_f32 v13, v13, v136\n"
    "v_max3_f32 v233, v233, v120, v121\n"
    "v_cmp_gt_u32 s[50:51], s45, v9\n"
    "v_mfma_f32_32x32x16_bf16 v[64:79], v[212:215], v[228:231], v[64:79]\n"
    "ds_read_b128 v[188:191], v7 offset:9728\n"
    "ds_write_b32 v12, v11\n"
    "v_add_f32 v14, v14, v137\n"
    "v_max3_f32 v232, v232, v122, v123\n"
    "v_add_f32 v13, v13, v138\n"
    "v_max3_f32 v233, v233, v124, v125\n"
    "v_max3_f32 v232, v232, v126, v127\n"
    "v_mfma_f32_32x32x16_bf16 v[80:95], v[216:219], v[228:231], v[80:95]\n"
    "v_max_f32 v232, v232, v233\n"
    "v_add_f32 v14, v14, v139\n"
    "v_cmp_lt_f32 vcc, s52, v232\n"
    "v_add_f32 v13, v13, v140\n"
    "v_add_f32 v14, v14, v141\n"
    "v_add_u32 v6, s44, v242\n"
    "v_mfma_f32_32x32x16_bf16 v[96:111], v[220:223], v[228:231], v[96:111]\n"
    "v_add_f32 v13, v13, v142\n"
    "v_add_f32 v14, v14, v143\n"
    "v_add_u32 v7, s44, v4\n"
    "s_or_b64 vcc, vcc, s[50:51]\n"
    "s_waitcnt lgkmcnt(1)\n"
    "s_cbranch_vccz .Lfa_aloop\n"
    "v_cmp_gt_u32 vcc, s45, v9\n"
    "s_cbranch_vccz .Lfa_lwd_8\n"
    ".Lfa_lw_7:\n"
    "ds_read_b32 v9, v10\n"
    "s_sub_u32 s62, s62, 1\n"
    "s_cselect_b32 s62, 0, s62\n"
    "s_waitcnt lgkmcnt(0)\n"
    "s_cbranch_scc1 .Lfa_lwd_8\n"
    "v_cmp_gt_u32 vcc, s45, v9\n"
    "s_cbranch_vccnz .Lfa_lw_7\n"
    ".Lfa_lwd_8:\n"
    "v_cmp_lt_f32 vcc, s52, v232\n"
    "s_cbranch_vccnz .Lfa_rareB\n"
    "s_branch .Lfa_aloop\n"
    ".Lfa_atail:\n"
    "ds_read_b64_tr_b16 v[196:197], v8 offset:8704\n"
    "ds_read_b64_tr_b16 v[198:199], v8 offset:10752\n"
    "ds_read_b64_tr_b16 v[212:213], v8 offset:12800\n"
    "ds_read_b64_tr_b16 v[214:215], v8 offset:14848\n"
    "ds_read_b64_tr_b16 v[200:201], v8 offset:9216\n"
    "ds_read_b64_tr_b16 v[202:203], v8 offset:11264\n"
    "ds_read_b64_tr_b16 v[216:217], v8 offset:13312\n"
    "ds_read_b64_tr_b16 v[218:219], v8 offset:15360\n"
    "ds_read_b64_tr_b16 v[204:205], v8 offset:9728\n"
    "ds_read_b64_tr_b16 v[206:207], v8 offset:11776\n"
    "ds_read_b64_tr_b16 v[220:221], v8 offset:13824\n"
    "ds_read_b64_tr_b16 v[222:223], v8 offset:15872\n"
    "ds_read_b64_tr_b16 v[192:193], v8 offset:8192\n"
    "ds_read_b64_tr_b16 v[194:195], v8 offset:10240\n"
    "ds_read_b64_tr_b16 v[208:209], v8 offset:12288\n"
    "ds_read_b64_tr_b16 v[210:211], v8 offset:14336\n"
    "v_exp_f32 v128, v128\n"
    "v_exp_f32 v129, v129\n"
    "v_exp_f32 v130, v130\n"
    "v_cvt_pk_bf16_f32 v224, v128, v129\n"
    "v_exp_f32 v131, v131\n"
    "v_exp_f32 v132, v132\n"
    "v_cvt_pk_bf16_f32 v225, v130, v131\n"
    "v_exp_f32 v133, v133\n"
    "v_exp_f32 v134, v134\n"
    "v_cvt_pk_bf16_f32 v226, v132, v133\n"
    "v_exp_f32 v135, v135\n"
    "v_exp_f32 v136, v136\n"
    "v_cvt_pk_bf16_f32 v227, v134, v135\n"
    "v_exp_f32 v137, v137\n"
    "v_exp_f32 v138, v138\n"
    "v_cvt_pk_bf16_f32 v228, v136, v137\n"
    "v_exp_f32 v139, v139\n"
    "v_exp_f32 v140, v140\n"
    "v_cvt_pk_bf16_f32 v229, v138, v139\n"
    "v_exp_f32 v141, v141\n"
    "v_exp_f32 v142, v142\n"
    "v_cvt_pk_bf16_f32 v230, v140, v141\n"
    "v_exp_f32 v143, v143\n"
    "s_mul_i32 s44, s48, 26624\n"
    "v_cvt_pk_bf16_f32 v231, v142, v143\n"
    "s_add_u32 s44, s44, 10485760\n"
    "s_waitcnt lgkmcnt(0)\n"
    "v_mfma_f32_32x32x16_bf16 v[64:79], v[196:199], v[224:227], v[64:79]\n"
    "v_add_f32 v13, v13, v128\n"
    "v_add_f32 v14, v14, v129\n"
    "v_add_f32 v13, v13, v130\n"
    "v_add_f32 v14, v14, v131\n"
    "v_add_f32 v13, v13, v132\n"
    "v_add_f32 v14, v14, v133\n"
    "v_mfma_f32_32x32x16_bf16 v[64:79], v[212:215], v[228:231], v[64:79]\n"
    "v_add_f32 v13, v13, v134\n"
    "v_add_f32 v14, v14, v135\n"
    "v_add_f32 v13, v13, v136\n"
    "v_add_f32 v14, v14, v137\n"
    "v_add_f32 v13, v13, v138\n"
    "v_add_f32 v14, v14, v139\n"
    "v_mfma_f32_32x32x16_bf16 v[80:95], v[200:203], v[224:227], v[80:95]\n"
    "v_add_f32 v13, v13, v140\n"
    "v_add_f32 v14, v14, v141\n"
    "v_add_f32 v13, v13, v142\n"
    "v_add_f32 v14, v14, v143\n"
    "v_add_f32 v13, v13, v14\n"
    "v_lshlrev_b32 v238, 3, v1\n"
    "v_mfma_f32_32x32x16_bf16 v[80:95], v[216:219], v[228:231], v[80:95]\n"
    "v_mov_b32 v14, v13\n"
    "s_xor_b32 s30, s23, 1\n"
    "s_mul_i32 s54, s30, 6144\n"
    "s_add_u32 s54, s54, s44\n"
    "s_add_u32 s54, s54, 0\n"
    "v_permlane32_swap_b32 v13, v14\n"
    "v_mfma_f32_32x32x16_bf16 v[96:111], v[204:207], v[224:227], v[96:111]\n"
    "v_cvt_pk_bf16_f32 v168, v64, v65\n"
    "v_cvt_pk_bf16_f32 v169, v66, v67\n"
    "v_cvt_pk_bf16_f32 v170, v68, v69\n"
    "v_cvt_pk_bf16_f32 v171, v70, v71\n"
    "v_add_f32 v14, v13, v14\n"
    "v_mfma_f32_32x32x16_bf16 v[96:111], v[220:223], v[228:231], v[96:111]\n"
    "buffer_store_dwordx4 v[168:171], v2, s[36:39], s54 offen offset:0 sc1\n"
    "v_cvt_pk_bf16_f32 v172, v72, v73\n"
    "v_cvt_pk_bf16_f32 v173, v74, v75\n"
    "v_cvt_pk_bf16_f32 v174, v76, v77\n"
    "v_cvt_pk_bf16_f32 v175, v78, v79\n"
    "v_mfma_f32_32x32x16_bf16 v[48:63], v[192:195], v[224:227], v[48:63]\n"
    "buffer_store_dwordx4 v[172:175], v2, s[36:39], s54 offen offset:1024 sc1\n"
    "s_lshl_b32 s30, s30, 9\n"
    "s_add_u32 s30, s30, s44\n"
    "s_add_u32 s57, s30, 24576\n"
    "s_xor_b32 s30, s23, 2\n"
    "s_mul_i32 s55, s30, 6144\n"
    "s_add_u32 s55, s55, s44\n"
    "s_add_u32 s55, s55, 2048\n"
    "v_cvt_pk_bf16_f32 v176, v80, v81\n"
    "v_cvt_pk_bf16_f32 v177, v82, v83\n"
    "v_cvt_pk_bf16_f32 v178, v84, v85\n"
    "v_cvt_pk_bf16_f32 v179, v86, v87\n"
    "v_mfma_f32_32x32x16_bf16 v[48:63], v[208:211], v[228:231], v[48:63]\n"
    "buffer_store_dwordx4 v[176:179], v2, s[36:39], s55 offen offset:0 sc1\n"
    "v_cvt_pk_bf16_f32 v180, v88, v89\n"
    "v_cvt_pk_bf16_f32 v181, v90, v91\n"
    "v_cvt_pk_bf16_f32 v182, v92, v93\n"
    "v_cvt_pk_bf16_f32 v183, v94, v95\n"
    "buffer_store_dwordx4 v[180:183], v2, s[36:39], s55 offen offset:1024 sc1\n"
    "s_lshl_b32 s30, s30, 9\n"
    "s_add_u32 s30, s30, s44\n"
    "s_add_u32 s58, s30, 24576\n"
    "s_xor_b32 s30, s23, 3\n"
    "s_mul_i32 s56, s30, 6144\n"
    "s_add_u32 s56, s56, s44\n"
    "s_add_u32 s56, s56, 4096\n"
    "v_cvt_pk_bf16_f32 v184, v96, v97\n"
    "v_cvt_pk_bf16_f32 v185, v98, v99\n"
    "v_cvt_pk_bf16_f32 v186, v100, v101\n"
    "v_cvt_pk_bf16_f32 v187, v102, v103\n"
    "buffer_store_dwordx4 v[184:187], v2, s[36:39], s56 offen offset:0 sc1\n"
    "v_cvt_pk_bf16_f32 v188, v104, v105\n"
    "v_cvt_pk_bf16_f32 v189, v106, v107\n"
    "v_cvt_pk_bf16_f32 v190, v108, v109\n"
    "v_cvt_pk_bf16_f32 v191, v110, v111\n"
    "buffer_store_dwordx4 v[188:191], v2, s[36:39], s56 offen offset:1024 sc1\n"
    "s_lshl_b32 s30, s30, 9\n"
    "s_add_u32 s30, s30, s44\n"
    "s_add_u32 s59, s30, 24576\n"
    "s_lshl_b32 s30, s23, 9\n"
    "s_add_u32 s30, s30, s44\n"
    "s_add_u32 s30, s30, 24576\n"
    "buffer_store_dwordx2 v[14:15], v238, s[36:39], s30 offen sc1\n"
    "s_mul_i32 s46, s23, 6144\n"
    "s_add_u32 s46, s46, s44\n"
    "s_add_u32 s47, s46, 4096\n"
    "s_lshl_b32 s45, s48, 9\n"
    "s_add_u32 s45, s45, 196608\n"
    "s_lshl_b32 s30, s23, 7\n"
    "s_add_u32 s44, s45, s30\n"
    "s_lshl_b32 s48, s48, 13\n"
    "s_lshl_b32 s30, s23, 11\n"
    "s_add_u32 s48, s48, s30\n"
    "s_add_u32 s48, s48, 8388608\n"
    "s_lshl_b32 s50, 1, s23\n"
    "s_xor_b32 s50, s50, 15\n"
    "s_mov_b32 s51, 0\n"
    "v_mov_b32 v234, s26\n"
    "v_mov_b32 v235, s27\n"
    "v_mov_b32 v236, s26\n"
    "v_mov_b32 v237, s27\n"
    "s_waitcnt vmcnt(0)\n"
    "s_mov_b64 exec, 0xff\n"
    "buffer_store_dwordx4 v[234:237], v2, s[36:39], s44 offen sc1\n"
    "s_mov_b64 exec, -1\n"
    "s_mov_b32 s49, 65536\n"
    "s_mov_b64 exec, s[50:51]\n"
    ".Lfa_fp_9:\n"
    "buffer_load_dwordx2 v[240:241], v244, s[36:39], s45 offen sc1\n"
    "s_sub_u32 s49, s49, 1\n"
    "s_cbranch_scc1 .Lfa_fpd_10\n"
    "s_waitcnt vmcnt(0)\n"
    "v_cmp_ne_u64 vcc, s[26:27], v[240:241]\n"
    "s_cbranch_vccnz .Lfa_fp_9\n"
    ".Lfa_fpd_10:\n"
    "s_mov_b64 exec, -1\n"
    "buffer_load_dwordx2 v[224:225], v238, s[36:39], s57 offen sc1\n"
    "buffer_load_dwordx2 v[226:227], v238, s[36:39], s58 offen sc1\n"
    "buffer_load_dwordx2 v[228:229], v238, s[36:39], s59 offen sc1\n"
    "buffer_load_dwordx4 v[192:195], v2, s[36:39], s46 offen offset:0 sc1\n"
    "buffer_load_dwordx4 v[200:203], v2, s[36:39], s46 offen offset:2048 sc1\n"
    "buffer_load_dwordx4 v[208:211], v2, s[36:39], s47 offen offset:0 sc1\n"
    "buffer_load_dwordx4 v[196:199], v2, s[36:39], s46 offen offset:1024 sc1\n"
    "buffer_load_dwordx4 v[204:207], v2, s[36:39], s46 offen offset:3072 sc1\n"
    "buffer_load_dwordx4 v[212:215], v2, s[36:39], s47 offen offset:1024 sc1\n"
    "s_add_u32 s44, s44, 131072\n"
    "s_waitcnt vmcnt(6)\n"
    "v_max3_f32 v232, v15, v225, v227\n"
    "v_max_f32 v232, v232, v229\n"
    "v_sub_f32 v246, v15, v232\n"
    "v_sub_f32 v247, v225, v232\n"
    "v_sub_f32 v248, v227, v232\n"
    "v_sub_f32 v249, v229, v232\n"
    "v_exp_f32 v246, v246\n"
    "v_exp_f32 v247, v247\n"
    "v_exp_f32 v248, v248\n"
    "v_exp_f32 v249, v249\n"
    "v_mul_f32 v13, v246, v14\n"
    "v_fmac_f32 v13, v247, v224\n"
    "v_fmac_f32 v13, v248, v226\n"
    "v_fmac_f32 v13, v249, v228\n"
    "v_rcp_f32 v13, v13\n"
    "s_nop 0\n"
    "v_mul_f32 v216, v246, v13\n"
    "v_mul_f32 v217, v246, v13\n"
    "v_mul_f32 v218, v247, v13\n"
    "v_mul_f32 v219, v247, v13\n"
    "v_mul_f32 v220, v248, v13\n"
    "v_mul_f32 v221, v248, v13\n"
    "v_mul_f32 v222, v249, v13\n"
    "v_mul_f32 v223, v249, v13\n"
    "v_pk_mul_f32 v[48:49], v[48:49], v[216:217]\n"
    "v_pk_mul_f32 v[50:51], v[50:51], v[216:217]\n"
    "v_pk_mul_f32 v[52:53], v[52:53], v[216:217]\n"
    "v_pk_mul_f32 v[54:55], v[54:55], v[216:217]\n"
    "s_waitcnt vmcnt(5)\n"
    "v_lshlrev_b32 v112, 16, v192\n"
    "v_and_b32 v113, 0xffff0000, v192\n"
    "v_lshlrev_b32 v114, 16, v193\n"
    "v_and_b32 v115, 0xffff0000, v193\n"
    "v_lshlrev_b32 v116, 16, v194\n"
    "v_and_b32 v117, 0xffff0000, v194\n"
    "v_lshlrev_b32 v118, 16, v195\n"
    "v_and_b32 v119, 0xffff0000, v195\n"
    "v_pk_fma_f32 v[48:49], v[112:113], v[218:219], v[48:49]\n"
    "v_pk_fma_f32 v[50:51], v[114:115], v[218:219], v[50:51]\n"
    "v_pk_fma_f32 v[52:53], v[116:117], v[218:219], v[52:53]\n"
    "v_pk_fma_f32 v[54:55], v[118:119], v[218:219], v[54:55]\n"
    "s_waitcnt vmcnt(4)\n"
    "v_lshlrev_b32 v128, 16, v200\n"
    "v_and_b32 v129, 0xffff0000, v200\n"
    "v_lshlrev_b32 v130, 16, v201\n"
    "v_and_b32 v131, 0xffff0000, v201\n"
    "v_lshlrev_b32 v132, 16, v202\n"
    "v_and_b32 v133, 0xffff0000, v202\n"
    "v_lshlrev_b32 v134, 16, v203\n"
    "v_and_b32 v135, 0xffff0000, v203\n"
    "v_pk_fma_f32 v[48:49], v[128:129], v[220:221], v[48:49]\n"
    "v_pk_fma_f32 v[50:51], v[130:131], v[220:221], v[50:51]\n"
    "v_pk_fma_f32 v[52:53], v[132:133], v[220:221], v[52:53]\n"
    "v_pk_fma_f32 v[54:55], v[134:135], v[220:221], v[54:55]\n"
    "s_waitcnt vmcnt(3)\n"
    "v_lshlrev_b32 v144, 16, v208\n"
    "v_and_b32 v145, 0xffff0000, v208\n"
    "v_lshlrev_b32 v146, 16, v209\n"
    "v_and_b32 v147, 0xffff0000, v209\n"
    "v_lshlrev_b32 v148, 16, v210\n"
    "v_and_b32 v149, 0xffff0000, v210\n"
    "v_lshlrev_b32 v150, 16, v211\n"
    "v_and_b32 v151, 0xffff0000, v211\n"
    "v_pk_fma_f32 v[48:49], v[144:145], v[222:223], v[48:49]\n"
    "v_pk_fma_f32 v[50:51], v[146:147], v[222:223], v[50:51]\n"
    "v_pk_fma_f32 v[52:53], v[148:149], v[222:223], v[52:53]\n"
    "v_pk_fma_f32 v[54:55], v[150:151], v[222:223], v[54:55]\n"
    "v_cvt_pk_bf16_f32 v160, v48, v49\n"
    "v_cvt_pk_bf16_f32 v161, v50, v51\n"
    "v_cvt_pk_bf16_f32 v162, v52, v53\n"
    "v_cvt_pk_bf16_f32 v163, v54, v55\n"
    "buffer_store_dwordx4 v[160:163], v2, s[36:39], s48 offen offset:0 sc1\n"
    "v_pk_mul_f32 v[56:57], v[56:57], v[216:217]\n"
    "v_pk_mul_f32 v[58:59], v[58:59], v[216:217]\n"
    "v_pk_mul_f32 v[60:61], v[60:61], v[216:217]\n"
    "v_pk_mul_f32 v[62:63], v[62:63], v[216:217]\n"
    "s_waitcnt vmcnt(3)\n"
    "v_lshlrev_b32 v120, 16, v196\n"
    "v_and_b32 v121, 0xffff0000, v196\n"
    "v_lshlrev_b32 v122, 16, v197\n"
    "v_and_b32 v123, 0xffff0000, v197\n"
    "v_lshlrev_b32 v124, 16, v198\n"
    "v_and_b32 v125, 0xffff0000, v198\n"
    "v_lshlrev_b32 v126, 16, v199\n"
    "v_and_b32 v127, 0xffff0000, v199\n"
    "v_pk_fma_f32 v[56:57], v[120:121], v[218:219], v[56:57]\n"
    "v_pk_fma_f32 v[58:59], v[122:123], v[218:219], v[58:59]\n"
    "v_pk_fma_f32 v[60:61], v[124:125], v[218:219], v[60:61]\n"
    "v_pk_fma_f32 v[62:63], v[126:127], v[218:219], v[62:63]\n"
    "s_waitcnt vmcnt(2)\n"
    "v_lshlrev_b32 v136, 16, v204\n"
    "v_and_b32 v137, 0xffff0000, v204\n"
    "v_lshlrev_b32 v138, 16, v205\n"
    "v_and_b32 v139, 0xffff0000, v205\n"
    "v_lshlrev_b32 v140, 16, v206\n"
    "v_and_b32 v141, 0xffff0000, v206\n"
    "v_lshlrev_b32 v142, 16, v207\n"
    "v_and_b32 v143, 0xffff0000, v207\n"
    "v_pk_fma_f32 v[56:57], v[136:137], v[220:221], v[56:57]\n"
    "v_pk_fma_f32 v[58:59], v[138:139], v[220:221], v[58:59]\n"
    "v_pk_fma_f32 v[60:61], v[140:141], v[220:221], v[60:61]\n"
    "v_pk_fma_f32 v[62:63], v[142:143], v[220:221], v[62:63]\n"
    "s_waitcnt vmcnt(1)\n"
    "v_lshlrev_b32 v152, 16, v212\n"
    "v_and_b32 v153, 0xffff0000, v212\n"
    "v_lshlrev_b32 v154, 16, v213\n"
    "v_and_b32 v155, 0xffff0000, v213\n"
    "v_lshlrev_b32 v156, 16, v214\n"
    "v_and_b32 v157, 0xffff0000, v214\n"
    "v_lshlrev_b32 v158, 16, v215\n"
    "v_and_b32 v159, 0xffff0000, v215\n"
    "v_pk_fma_f32 v[56:57], v[152:153], v[222:223], v[56:57]\n"
    "v_pk_fma_f32 v[58:59], v[154:155], v[222:223], v[58:59]\n"
    "v_pk_fma_f32 v[60:61], v[156:157], v[222:223], v[60:61]\n"
    "v_pk_fma_f32 v[62:63], v[158:159], v[222:223], v[62:63]\n"
    "v_cvt_pk_bf16_f32 v164, v56, v57\n"
    "v_cvt_pk_bf16_f32 v165, v58, v59\n"
    "v_cvt_pk_bf16_f32 v166, v60, v61\n"
    "v_cvt_pk_bf16_f32 v167, v62, v63\n"
    "buffer_store_dwordx4 v[164:167], v2, s[36:39], s48 offen offset:1024 sc1\n"
    "v_mov_b32 v234, s26\n"
    "v_mov_b32 v235, s27\n"
    "v_mov_b32 v236, s26\n"
    "v_mov_b32 v237, s27\n"
    "s_waitcnt vmcnt(0)\n"
    "s_mov_b64 exec, 0xff\n"
    "buffer_store_dwordx4 v[234:237], v2, s[36:39], s44 offen sc1\n"
    "s_mov_b64 exec, -1\n"
    "s_barrier\n"
    "s_endpgm\n"
    ".Lfa_rareA:\n"
    "v_mov_b32 v233, v232\n"
    "s_nop 1\n"
    "v_permlane32_swap_b32 v232, v233\n"
    "v_max_f32 v232, v232, v233\n"
    "v_ceil_f32 v232, v232\n"
    "v_max_f32 v232, 0, v232\n"
    "v_add_f32 v15, v15, v232\n"
    "v_sub_f32 v233, 0, v232\n"
    "v_exp_f32 v233, v233\n"
    "s_nop 7\n"
    "v_sub_f32 v128, v128, v232\n"
    "v_sub_f32 v129, v129, v232\n"
    "v_sub_f32 v130, v130, v232\n"
    "v_sub_f32 v131, v131, v232\n"
    "v_sub_f32 v132, v132, v232\n"
    "v_sub_f32 v133, v133, v232\n"
    "v_sub_f32 v134, v134, v232\n"
    "v_sub_f32 v135, v135, v232\n"
    "v_sub_f32 v136, v136, v232\n"
    "v_sub_f32 v137, v137, v232\n"
    "v_sub_f32 v138, v138, v232\n"
    "v_sub_f32 v139, v139, v232\n"
    "v_sub_f32 v140, v140, v232\n"
    "v_sub_f32 v141, v141, v232\n"
    "v_sub_f32 v142, v142, v232\n"
    "v_sub_f32 v143, v143, v232\n"
    "v_sub_f32 v144, 0, v15\n"
    "v_sub_f32 v145, 0, v15\n"
    "v_sub_f32 v146, 0, v15\n"
    "v_sub_f32 v147, 0, v15\n"
    "v_sub_f32 v148, 0, v15\n"
    "v_sub_f32 v149, 0, v15\n"
    "v_sub_f32 v150, 0, v15\n"
    "v_sub_f32 v151, 0, v15\n"
    "v_sub_f32 v152, 0, v15\n"
    "v_sub_f32 v153, 0, v15\n"
    "v_sub_f32 v154, 0, v15\n"
    "v_sub_f32 v155, 0, v15\n"
    "v_sub_f32 v156, 0, v15\n"
    "v_sub_f32 v157, 0, v15\n"
    "v_sub_f32 v158, 0, v15\n"
    "v_sub_f32 v159, 0, v15\n"
    "v_mul_f32 v48, v48, v233\n"
    "v_mul_f32 v49, v49, v233\n"
    "v_mul_f32 v50, v50, v233\n"
    "v_mul_f32 v51, v51, v233\n"
    "v_mul_f32 v52, v52, v233\n"
    "v_mul_f32 v53, v53, v233\n"
    "v_mul_f32 v54, v54, v233\n"
    "v_mul_f32 v55, v55, v233\n"
    "v_mul_f32 v56, v56, v233\n"
    "v_mul_f32 v57, v57, v233\n"
    "v_mul_f32 v58, v58, v233\n"
    "v_mul_f32 v59, v59, v233\n"
    "v_mul_f32 v60, v60, v233\n"
    "v_mul_f32 v61, v61, v233\n"
    "v_mul_f32 v62, v62, v233\n"
    "v_mul_f32 v63, v63, v233\n"
    "v_mul_f32 v64, v64, v233\n"
    "v_mul_f32 v65, v65, v233\n"
    "v_mul_f32 v66, v66, v233\n"
    "v_mul_f32 v67, v67, v233\n"
    "v_mul_f32 v68, v68, v233\n"
    "v_mul_f32 v69, v69, v233\n"
    "v_mul_f32 v70, v70, v233\n"
    "v_mul_f32 v71, v71, v233\n"
    "v_mul_f32 v72, v72, v233\n"
    "v_mul_f32 v73, v73, v233\n"
    "v_mul_f32 v74, v74, v233\n"
    "v_mul_f32 v75, v75, v233\n"
    "v_mul_f32 v76, v76, v233\n"
    "v_mul_f32 v77, v77, v233\n"
    "v_mul_f32 v78, v78, v233\n"
    "v_mul_f32 v79, v79, v233\n"
    "v_mul_f32 v80, v80, v233\n"
    "v_mul_f32 v81, v81, v233\n"
    "v_mul_f32 v82, v82, v233\n"
    "v_mul_f32 v83, v83, v233\n"
    "v_mul_f32 v84, v84, v233\n"
    "v_mul_f32 v85, v85, v233\n"
    "v_mul_f32 v86, v86, v233\n"
    "v_mul_f32 v87, v87, v233\n"
    "v_mul_f32 v88, v88, v233\n"
    "v_mul_f32 v89, v89, v233\n"
    "v_mul_f32 v90, v90, v233\n"
    "v_mul_f32 v91, v91, v233\n"
    "v_mul_f32 v92, v92, v233\n"
    "v_mul_f32 v93, v93, v233\n"
    "v_mul_f32 v94, v94, v233\n"
    "v_mul_f32 v95, v95, v233\n"
    "v_mul_f32 v96, v96, v233\n"
    "v_mul_f32 v97, v97, v233\n"
    "v_mul_f32 v98, v98, v233\n"
    "v_mul_f32 v99, v99, v233\n"
    "v_mul_f32 v100, v100, v233\n"
    "v_mul_f32 v101, v101, v233\n"
    "v_mul_f32 v102, v102, v233\n"
    "v_mul_f32 v103, v103, v233\n"
    "v_mul_f32 v104, v104, v233\n"
    "v_mul_f32 v105, v105, v233\n"
    "v_mul_f32 v106, v106, v233\n"
    "v_mul_f32 v107, v107, v233\n"
    "v_mul_f32 v108, v108, v233\n"
    "v_mul_f32 v109, v109, v233\n"
    "v_mul_f32 v110, v110, v233\n"
    "v_mul_f32 v111, v111, v233\n"
    "v_mul_f32 v13, v13, v233\n"
    "v_mul_f32 v14, v14, v233\n"
    "s_nop 1\n"
    "s_branch .Lfa_backA\n"
    ".Lfa_rareB:\n"
    "v_mov_b32 v233, v232\n"
    "s_nop 1\n"
    "v_permlane32_swap_b32 v232, v233\n"
    "v_max_f32 v232, v232, v233\n"
    "v_ceil_f32 v232, v232\n"
    "v_max_f32 v232, 0, v232\n"
    "v_add_f32 v15, v15, v232\n"
    "v_sub_f32 v233, 0, v232\n"
    "v_exp_f32 v233, v233\n"
    "s_nop 7\n"
    "v_sub_f32 v112, v112, v232\n"
    "v_sub_f32 v113, v113, v232\n"
    "v_sub_f32 v114, v114, v232\n"
    "v_sub_f32 v115, v115, v232\n"
    "v_sub_f32 v116, v116, v232\n"
    "v_sub_f32 v117, v117, v232\n"
    "v_sub_f32 v118, v118, v232\n"
    "v_sub_f32 v119, v119, v232\n"
    "v_sub_f32 v120, v120, v232\n"
    "v_sub_f32 v121, v121, v232\n"
    "v_sub_f32 v122, v122, v232\n"
    "v_sub_f32 v123, v123, v232\n"
    "v_sub_f32 v124, v124, v232\n"
    "v_sub_f32 v125, v125, v232\n"
    "v_sub_f32 v126, v126, v232\n"
    "v_sub_f32 v127, v127, v232\n"
    "v_sub_f32 v144, 0, v15\n"
    "v_sub_f32 v145, 0, v15\n"
    "v_sub_f32 v146, 0, v15\n"
    "v_sub_f32 v147, 0, v15\n"
    "v_sub_f32 v148, 0, v15\n"
    "v_sub_f32 v149, 0, v15\n"
    "v_sub_f32 v150, 0, v15\n"
    "v_sub_f32 v151, 0, v15\n"
    "v_sub_f32 v152, 0, v15\n"
    "v_sub_f32 v153, 0, v15\n"
    "v_sub_f32 v154, 0, v15\n"
    "v_sub_f32 v155, 0, v15\n"
    "v_sub_f32 v156, 0, v15\n"
    "v_sub_f32 v157, 0, v15\n"
    "v_sub_f32 v158, 0, v15\n"
    "v_sub_f32 v159, 0, v15\n"
    "v_mul_f32 v48, v48, v233\n"
    "v_mul_f32 v49, v49, v233\n"
    "v_mul_f32 v50, v50, v233\n"
    "v_mul_f32 v51, v51, v233\n"
    "v_mul_f32 v52, v52, v233\n"
    "v_mul_f32 v53, v53, v233\n"
    "v_mul_f32 v54, v54, v233\n"
    "v_mul_f32 v55, v55, v233\n"
    "v_mul_f32 v56, v56, v233\n"
    "v_mul_f32 v57, v57, v233\n"
    "v_mul_f32 v58, v58, v233\n"
    "v_mul_f32 v59, v59, v233\n"
    "v_mul_f32 v60, v60, v233\n"
    "v_mul_f32 v61, v61, v233\n"
    "v_mul_f32 v62, v62, v233\n"
    "v_mul_f32 v63, v63, v233\n"
    "v_mul_f32 v64, v64, v233\n"
    "v_mul_f32 v65, v65, v233\n"
    "v_mul_f32 v66, v66, v233\n"
    "v_mul_f32 v67, v67, v233\n"
    "v_mul_f32 v68, v68, v233\n"
    "v_mul_f32 v69, v69, v233\n"
    "v_mul_f32 v70, v70, v233\n"
    "v_mul_f32 v71, v71, v233\n"
    "v_mul_f32 v72, v72, v233\n"
    "v_mul_f32 v73, v73, v233\n"
    "v_mul_f32 v74, v74, v233\n"
    "v_mul_f32 v75, v75, v233\n"
    "v_mul_f32 v76, v76, v233\n"
    "v_mul_f32 v77, v77, v233\n"
    "v_mul_f32 v78, v78, v233\n"
    "v_mul_f32 v79, v79, v233\n"
    "v_mul_f32 v80, v80, v233\n"
    "v_mul_f32 v81, v81, v233\n"
    "v_mul_f32 v82, v82, v233\n"
    "v_mul_f32 v83, v83, v233\n"
    "v_mul_f32 v84, v84, v233\n"
    "v_mul_f32 v85, v85, v233\n"
    "v_mul_f32 v86, v86, v233\n"
    "v_mul_f32 v87, v87, v233\n"
    "v_mul_f32 v88, v88, v233\n"
    "v_mul_f32 v89, v89, v233\n"
    "v_mul_f32 v90, v90, v233\n"
    "v_mul_f32 v91, v91, v233\n"
    "v_mul_f32 v92, v92, v233\n"
    "v_mul_f32 v93, v93, v233\n"
    "v_mul_f32 v94, v94, v233\n"
    "v_mul_f32 v95, v95, v233\n"
    "v_mul_f32 v96, v96, v233\n"
    "v_mul_f32 v97, v97, v233\n"
    "v_mul_f32 v98, v98, v233\n"
    "v_mul_f32 v99, v99, v233\n"
    "v_mul_f32 v100, v100, v233\n"
    "v_mul_f32 v101, v101, v233\n"
    "v_mul_f32 v102, v102, v233\n"
    "v_mul_f32 v103, v103, v233\n"
    "v_mul_f32 v104, v104, v233\n"
    "v_mul_f32 v105, v105, v233\n"
    "v_mul_f32 v106, v106, v233\n"
    "v_mul_f32 v107, v107, v233\n"
    "v_mul_f32 v108, v108, v233\n"
    "v_mul_f32 v109, v109, v233\n"
    "v_mul_f32 v110, v110, v233\n"
    "v_mul_f32 v111, v111, v233\n"
    "v_mul_f32 v13, v13, v233\n"
    "v_mul_f32 v14, v14, v233\n"
    "s_nop 1\n"
    "s_branch .Lfa_aloop\n"
    ".Lfa_B:\n"
    "v_bfe_u32 v240, v1, 2, 3\n"
    "v_lshrrev_b32 v244, 5, v1\n"
    "v_lshlrev_b32 v244, 6, v244\n"
    "v_and_b32 v245, 3, v1\n"
    "v_lshl_add_u32 v241, v245, 4, v244\n"
    "v_and_b32 v246, 16, v1\n"
    "v_xor_b32 v242, v246, v241\n"
    "s_add_u32 s46, s24, s25\n"
    "s_and_b32 s46, s46, 3\n"
    "s_lshl_b32 s46, s46, 2\n"
    "s_and_b32 s35, s18, 1\n"
    "s_add_u32 s46, s46, s35\n"
    "s_lshl_b32 s45, s35, 13\n"
    "s_mov_b32 s43, 0x20000\n"
    "s_cmp_ge_u32 s18, 6\n"
    "s_cbranch_scc1 .Lfa_hwq\n"
    "v_lshl_add_u32 v4, v240, 11, v242\n"
    "v_xor_b32 v5, 32, v4\n"
    "v_add_u32 v6, 30720, v4\n"
    "v_add_u32 v7, 46080, v5\n"
    "v_add_u32 v5, 15360, v5\n"
    "s_lshl_b32 s47, s24, 17\n"
    "s_mov_b32 s48, 7\n"
    "s_mov_b32 s49, 65536\n"
    "s_mov_b32 s50, 32768\n"
    "s_mov_b32 s42, 524288\n"
    "s_waitcnt lgkmcnt(0)\n"
    "s_mov_b32 s40, s4\n"
    "s_and_b32 s41, s5, 0xffff\n"
    "s_branch .Lfa_hq\n"
    ".Lfa_hwq:\n"
    "v_lshl_add_u32 v4, v240, 13, v241\n"
    "v_add_u32 v5, 64512, v4\n"
    "v_add_u32 v6, 129024, v4\n"
    "v_add_u32 v7, 193536, v4\n"
    "s_lshl_b32 s47, s19, 10\n"
    "s_lshl_b32 s34, s25, 7\n"
    "s_add_u32 s47, s47, s34\n"
    "s_mov_b32 s48, 19\n"
    "s_mov_b32 s49, 262144\n"
    "s_mov_b32 s50, 81920\n"
    "s_mov_b32 s42, 8388608\n"
    "s_waitcnt lgkmcnt(0)\n"
    "s_mov_b32 s40, s10\n"
    "s_and_b32 s41, s11, 0xffff\n"
    ".Lfa_hq:\n"
    "s_lshl_b32 s44, s46, s48\n"
    "s_add_u32 s44, s44, s47\n"
    "s_add_u32 s35, s45, s50\n"
    "s_mov_b32 m0, s35\n"
    "s_add_u32 s34, s44, s49\n"
    "buffer_load_dwordx4 v4, s[40:43], s44 offen offset:0  lds\n"
    "buffer_load_dwordx4 v5, s[40:43], s44 offen offset:1024  lds\n"
    "buffer_load_dwordx4 v6, s[40:43], s44 offen offset:2048  lds\n"
    "buffer_load_dwordx4 v7, s[40:43], s44 offen offset:3072  lds\n"
    "s_add_u32 m0, s35, 4096\n"
    "s_nop 0\n"
    "buffer_load_dwordx4 v4, s[40:43], s34 offen offset:0  lds\n"
    "buffer_load_dwordx4 v5, s[40:43], s34 offen offset:1024  lds\n"
    "buffer_load_dwordx4 v6, s[40:43], s34 offen offset:2048  lds\n"
    "buffer_load_dwordx4 v7, s[40:43], s34 offen offset:3072  lds\n"
    "s_add_u32 s46, s46, 2\n"
    "s_and_b32 s46, s46, 15\n"
    "s_add_u32 s45, s45, 16384\n"
    "s_cmp_ge_u32 s45, 49152\n"
    "s_cselect_b32 s34, 49152, 0\n"
    "s_sub_u32 s45, s45, s34\n"
    "s_load_dwordx2 s[26:27], s[16:17], 0\n"
    "s_mov_b32 s36, s16\n"
    "s_and_b32 s37, s17, 0xffff\n"
    "s_mov_b32 s38, 25165824\n"
    "s_mov_b32 s39, 0x20000\n"
    "s_mov_b32 s56, s6\n"
    "s_and_b32 s57, s7, 0xffff\n"
    "s_mov_b32 s58, 67108864\n"
    "s_mov_b32 s59, 0x20000\n"
    "s_mov_b32 s28, s8\n"
    "s_and_b32 s29, s9, 0xffff\n"
    "s_mov_b32 s30, 67108864\n"
    "s_mov_b32 s31, 0x20000\n"
    "v_lshl_add_u32 v17, v240, 8, v242\n"
    "v_xor_b32 v19, 32, v17\n"
    "v_add_u32 v16, 1024, v17\n"
    "v_add_u32 v18, 1024, v19\n"
    "v_add_u32 v17, 128, v17\n"
    "v_add_u32 v19, 128, v19\n"
    "v_lshl_add_u32 v9, v240, 8, v241\n"
    "s_lshl_b32 s34, s23, 6\n"
    "v_xor_b32 v9, s34, v9\n"
    "v_add_u32 v8, 1024, v9\n"
    "v_xor_b32 v9, 128, v9\n"
    "s_lshl_b32 s55, s21, 2\n"
    "s_lshr_b32 s34, s19, 1\n"
    "s_add_u32 s55, s55, s34\n"
    "s_lshl_b32 s55, s55, 21\n"
    "s_lshl_b32 s34, s23, 17\n"
    "s_add_u32 s55, s55, s34\n"
    "s_add_u32 s55, s55, 1571840\n"
    "s_mov_b32 s52, s55\n"
    "s_mov_b32 s53, 65536\n"
    "v_and_b32 v11, 3, v1\n"
    "v_lshlrev_b32 v11, 8, v11\n"
    "v_lshl_add_u32 v11, v1, 2, v11\n"
    "v_add_u32 v11, 131584, v11\n"
    "v_mov_b32 v10, 0\n"
    "v_lshlrev_b32 v13, 2, v1\n"
    "v_add_u32 v13, 131072, v13\n"
    "v_lshlrev_b32 v3, 7, v1\n"
    "s_mov_b32 s51, 0\n"
    ".Lfa_hqloop:\n"
    "s_lshl_b32 s44, s46, s48\n"
    "s_add_u32 s44, s44, s47\n"
    "s_add_u32 s35, s45, s50\n"
    "s_mov_b32 m0, s35\n"
    "s_add_u32 s34, s44, s49\n"
    "buffer_load_dwordx4 v4, s[40:43], s44 offen offset:0  lds\n"
    "buffer_load_dwordx4 v5, s[40:43], s44 offen offset:1024  lds\n"
    "buffer_load_dwordx4 v6, s[40:43], s44 offen offset:2048  lds\n"
    "buffer_load_dwordx4 v7, s[40:43], s44 offen offset:3072  lds\n"
    "s_add_u32 m0, s35, 4096\n"
    "s_nop 0\n"
    "buffer_load_dwordx4 v4, s[40:43], s34 offen offset:0  lds\n"
    "buffer_load_dwordx4 v5, s[40:43], s34 offen offset:1024  lds\n"
    "buffer_load_dwordx4 v6, s[40:43], s34 offen offset:2048  lds\n"
    "buffer_load_dwordx4 v7, s[40:43], s34 offen offset:3072  lds\n"
    "s_add_u32 s46, s46, 2\n"
    "s_and_b32 s46, s46, 15\n"
    "s_add_u32 s45, s45, 16384\n"
    "s_cmp_ge_u32 s45, 49152\n"
    "s_cselect_b32 s34, 49152, 0\n"
    "s_sub_u32 s45, s45, s34\n"
    "s_add_u32 s51, s51, 1\n"
    "s_cmp_lt_u32 s51, 7\n"
    "s_waitcnt vmcnt(8) lgkmcnt(0)\n"
    "s_barrier\n"
    "s_cbranch_scc1 .Lfa_hqloop\n"
    "s_cmp_lg_u32 s18, 4\n"
    "s_cbranch_scc1 .Lfa_hidle\n"
    "s_mov_b32 s45, 0\n"
    "s_mov_b32 s51, 1\n"
    "s_add_u32 m0, s45, 0\n"
    "s_nop 0\n"
    "buffer_load_dwordx4 v16, s[56:59], s55 offen offset:0  lds\n"
    "buffer_load_dwordx4 v17, s[56:59], s55 offen offset:1024  lds\n"
    "buffer_load_dwordx4 v18, s[56:59], s55 offen offset:2048  lds\n"
    "buffer_load_dwordx4 v19, s[56:59], s55 offen offset:3072  lds\n"
    "s_add_u32 s55, s55, 4096\n"
    "s_add_u32 m0, s45, 4096\n"
    "s_nop 0\n"
    "buffer_load_dwordx4 v16, s[56:59], s55 offen offset:0  lds\n"
    "buffer_load_dwordx4 v17, s[56:59], s55 offen offset:1024  lds\n"
    "buffer_load_dwordx4 v18, s[56:59], s55 offen offset:2048  lds\n"
    "buffer_load_dwordx4 v19, s[56:59], s55 offen offset:3072  lds\n"
    "s_add_u32 s55, s55, 4096\n"
    "s_add_u32 m0, s45, 8192\n"
    "s_nop 0\n"
    "buffer_load_dwordx4 v16, s[56:59], s55 offen offset:0  lds\n"
    "buffer_load_dwordx4 v17, s[56:59], s55 offen offset:1024  lds\n"
    "buffer_load_dwordx4 v18, s[56:59], s55 offen offset:2048  lds\n"
    "buffer_load_dwordx4 v19, s[56:59], s55 offen offset:3072  lds\n"
    "s_add_u32 s55, s55, 4096\n"
    "s_add_u32 m0, s45, 12288\n"
    "s_nop 0\n"
    "buffer_load_dwordx4 v16, s[56:59], s55 offen offset:0  lds\n"
    "buffer_load_dwordx4 v17, s[56:59], s55 offen offset:1024  lds\n"
    "buffer_load_dwordx4 v18, s[56:59], s55 offen offset:2048  lds\n"
    "buffer_load_dwordx4 v19, s[56:59], s55 offen offset:3072  lds\n"
    "s_add_u32 s55, s55, 4096\n"
    "s_waitcnt vmcnt(16)\n"
    "s_barrier\n"
    "s_mov_b32 s45, 16384\n"
    ".Lfa_kvloop:\n"
    "s_cmp_eq_u32 s51, 8\n"
    "s_cbranch_scc1 .Lfa_kvv\n"
    "s_add_u32 m0, s45, 0\n"
    "s_nop 0\n"
    "buffer_load_dwordx4 v16, s[56:59], s55 offen offset:0  lds\n"
    "buffer_load_dwordx4 v17, s[56:59], s55 offen offset:1024  lds\n"
    "buffer_load_dwordx4 v18, s[56:59], s55 offen offset:2048  lds\n"
    "buffer_load_dwordx4 v19, s[56:59], s55 offen offset:3072  lds\n"
    "s_add_u32 s55, s55, 4096\n"
    "s_add_u32 m0, s45, 4096\n"
    "s_nop 0\n"
    "buffer_load_dwordx4 v16, s[56:59], s55 offen offset:0  lds\n"
    "buffer_load_dwordx4 v17, s[56:59], s55 offen offset:1024  lds\n"
    "buffer_load_dwordx4 v18, s[56:59], s55 offen offset:2048  lds\n"
    "buffer_load_dwordx4 v19, s[56:59], s55 offen offset:3072  lds\n"
    "s_add_u32 s55, s55, 4096\n"
    "s_add_u32 m0, s45, 8192\n"
    "s_nop 0\n"
    "buffer_load_dwordx4 v16, s[56:59], s55 offen offset:0  lds\n"
    "buffer_load_dwordx4 v17, s[56:59], s55 offen offset:1024  lds\n"
    "buffer_load_dwordx4 v18, s[56:59], s55 offen offset:2048  lds\n"
    "buffer_load_dwordx4 v19, s[56:59], s55 offen offset:3072  lds\n"
    "s_add_u32 s55, s55, 4096\n"
    "s_add_u32 m0, s45, 12288\n"
    "s_nop 0\n"
    "buffer_load_dwordx4 v16, s[56:59], s55 offen offset:0  lds\n"
    "buffer_load_dwordx4 v17, s[56:59], s55 offen offset:1024  lds\n"
    "buffer_load_dwordx4 v18, s[56:59], s55 offen offset:2048  lds\n"
    "buffer_load_dwordx4 v19, s[56:59], s55 offen offset:3072  lds\n"
    "s_add_u32 s55, s55, 4096\n"
    "s_add_u32 s45, s45, 16384\n"
    "s_and_b32 s45, s45, 65535\n"
    ".Lfa_kvv:\n"
    "s_add_u32 m0, s53, 0\n"
    "s_nop 0\n"
    "buffer_load_dwordx4 v8, s[28:31], s52 offen offset:0  lds\n"
    "buffer_load_dwordx4 v9, s[28:31], s52 offen offset:1024  lds\n"
    "buffer_load_dwordx4 v8, s[28:31], s52 offen offset:2048  lds\n"
    "buffer_load_dwordx4 v9, s[28:31], s52 offen offset:3072  lds\n"
    "s_add_u32 s52, s52, 4096\n"
    "s_add_u32 m0, s53, 4096\n"
    "s_nop 0\n"
    "buffer_load_dwordx4 v8, s[28:31], s52 offen offset:0  lds\n"
    "buffer_load_dwordx4 v9, s[28:31], s52 offen offset:1024  lds\n"
    "buffer_load_dwordx4 v8, s[28:31], s52 offen offset:2048  lds\n"
    "buffer_load_dwordx4 v9, s[28:31], s52 offen offset:3072  lds\n"
    "s_add_u32 s52, s52, 4096\n"
    "s_add_u32 m0, s53, 8192\n"
    "s_nop 0\n"
    "buffer_load_dwordx4 v8, s[28:31], s52 offen offset:0  lds\n"
    "buffer_load_dwordx4 v9, s[28:31], s52 offen offset:1024  lds\n"
    "buffer_load_dwordx4 v8, s[28:31], s52 offen offset:2048  lds\n"
    "buffer_load_dwordx4 v9, s[28:31], s52 offen offset:3072  lds\n"
    "s_add_u32 s52, s52, 4096\n"
    "s_add_u32 m0, s53, 12288\n"
    "s_nop 0\n"
    "buffer_load_dwordx4 v8, s[28:31], s52 offen offset:0  lds\n"
    "buffer_load_dwordx4 v9, s[28:31], s52 offen offset:1024  lds\n"
    "buffer_load_dwordx4 v8, s[28:31], s52 offen offset:2048  lds\n"
    "buffer_load_dwordx4 v9, s[28:31], s52 offen offset:3072  lds\n"
    "s_add_u32 s52, s52, 4096\n"
    "s_add_u32 s53, s53, 16384\n"
    "s_and_b32 s53, s53, 65535\n"
    "s_or_b32 s53, s53, 65536\n"
    "s_cmp_eq_u32 s51, 8\n"
    "s_cbranch_scc1 .Lfa_kvdone\n"
    "s_waitcnt vmcnt(32)\n"
    "v_mov_b32 v12, s51\n"
    "ds_write_b32 v13, v12\n"
    "s_add_u32 s51, s51, 1\n"
    ".Lfa_kvchk:\n"
    "s_sub_u32 s54, s51, 2\n"
    "s_max_i32 s54, s54, 1\n"
    "v_cmp_gt_u32 vcc, s54, v10\n"
    "s_cbranch_vccz .Lfa_kvloop\n"
    "ds_read_b32 v10, v11\n"
    "s_waitcnt lgkmcnt(0)\n"
    "v_cmp_gt_u32 vcc, s54, v10\n"
    "s_cbranch_vccz .Lfa_kvloop\n"
    "s_waitcnt vmcnt(0)\n"
    "v_mov_b32 v12, s51\n"
    "ds_write_b32 v13, v12\n"
    ".Lfa_lw_11:\n"
    "ds_read_b32 v10, v11\n"
    "s_sub_u32 s62, s62, 1\n"
    "s_cselect_b32 s62, 0, s62\n"
    "s_waitcnt lgkmcnt(0)\n"
    "s_cbranch_scc1 .Lfa_lwd_12\n"
    "v_cmp_gt_u32 vcc, s54, v10\n"
    "s_cbranch_vccnz .Lfa_lw_11\n"
    ".Lfa_lwd_12:\n"
    "s_branch .Lfa_kvloop\n"
    ".Lfa_kvdone:\n"
    "s_mov_b32 s40, s12\n"
    "s_and_b32 s41, s13, 0xffff\n"
    "s_mov_b32 s42, 8388608\n"
    "s_mov_b32 s43, 0x20000\n"
    "v_lshl_add_u32 v4, v240, 11, v241\n"
    "v_add_u32 v5, 15360, v4\n"
    "v_add_u32 v6, 30720, v4\n"
    "v_add_u32 v7, 46080, v4\n"
    "s_lshl_b32 s44, s19, 20\n"
    "s_and_b32 s34, s20, 15\n"
    "s_lshl_b32 s34, s34, 7\n"
    "s_add_u32 s44, s44, s34\n"
    "s_mov_b32 s45, 0\n"
    "s_mov_b32 s54, 6\n"
    ".Lfa_woloop:\n"
    "s_mov_b32 m0, s45\n"
    "s_add_u32 s34, s44, 65536\n"
    "buffer_load_dwordx4 v4, s[40:43], s44 offen offset:0  lds\n"
    "buffer_load_dwordx4 v5, s[40:43], s44 offen offset:1024  lds\n"
    "buffer_load_dwordx4 v6, s[40:43], s44 offen offset:2048  lds\n"
    "buffer_load_dwordx4 v7, s[40:43], s44 offen offset:3072  lds\n"
    "s_add_u32 m0, s45, 4096\n"
    "s_nop 0\n"
    "buffer_load_dwordx4 v4, s[40:43], s34 offen offset:0  lds\n"
    "buffer_load_dwordx4 v5, s[40:43], s34 offen offset:1024  lds\n"
    "buffer_load_dwordx4 v6, s[40:43], s34 offen offset:2048  lds\n"
    "buffer_load_dwordx4 v7, s[40:43], s34 offen offset:3072  lds\n"
    "s_add_u32 s44, s44, 131072\n"
    "s_add_u32 s45, s45, 8192\n"
    "s_mov_b32 m0, s45\n"
    "s_add_u32 s34, s44, 65536\n"
    "buffer_load_dwordx4 v4, s[40:43], s44 offen offset:0  lds\n"
    "buffer_load_dwordx4 v5, s[40:43], s44 offen offset:1024  lds\n"
    "buffer_load_dwordx4 v6, s[40:43], s44 offen offset:2048  lds\n"
    "buffer_load_dwordx4 v7, s[40:43], s44 offen offset:3072  lds\n"
    "s_add_u32 m0, s45, 4096\n"
    "s_nop 0\n"
    "buffer_load_dwordx4 v4, s[40:43], s34 offen offset:0  lds\n"
    "buffer_load_dwordx4 v5, s[40:43], s34 offen offset:1024  lds\n"
    "buffer_load_dwordx4 v6, s[40:43], s34 offen offset:2048  lds\n"
    "buffer_load_dwordx4 v7, s[40:43], s34 offen offset:3072  lds\n"
    "s_add_u32 s44, s44, 131072\n"
    "s_add_u32 s45, s45, 8192\n"
    "s_cmp_lg_u32 s54, 6\n"
    "s_cbranch_scc1 .Lfa_wo1\n"
    "s_waitcnt vmcnt(32)\n"
    "v_mov_b32 v12, 8\n"
    "ds_write_b32 v13, v12\n"
    "s_waitcnt vmcnt(16)\n"
    "v_mov_b32 v12, 9\n"
    "ds_write_b32 v13, v12\n"
    ".Lfa_wo1:\n"
    "s_add_u32 s54, s54, 1\n"
    "s_cmp_eq_u32 s54, 10\n"
    "s_cbranch_scc1 .Lfa_wodone\n"
    "v_cmp_gt_u32 vcc, s54, v10\n"
    "s_cbranch_vccz .Lfa_lwd_14\n"
    ".Lfa_lw_13:\n"
    "s_sleep 1\n"
    "ds_read_b32 v10, v11\n"
    "s_sub_u32 s62, s62, 1\n"
    "s_cselect_b32 s62, 0, s62\n"
    "s_waitcnt lgkmcnt(0)\n"
    "s_cbranch_scc1 .Lfa_lwd_14\n"
    "v_cmp_gt_u32 vcc, s54, v10\n"
    "s_cbranch_vccnz .Lfa_lw_13\n"
    ".Lfa_lwd_14:\n"
    "s_branch .Lfa_woloop\n"
    ".Lfa_wodone:\n"
    "s_mul_hi_u32 s34, s26, 1284865837\n"
    "s_mul_i32 s35, s26, 1481765933\n"
    "s_add_u32 s34, s34, s35\n"
    "s_mul_i32 s35, s27, 1284865837\n"
    "s_add_u32 s34, s34, s35\n"
    "s_mul_i32 s26, s26, 1284865837\n"
    "s_add_u32 s26, s26, 4150755663\n"
    "s_addc_u32 s27, s34, 335903614\n"
    "s_waitcnt vmcnt(0)\n"
    "v_mov_b32 v12, 10\n"
    "ds_write_b32 v13, v12\n"
    "s_branch .Lfa_hout\n"
    ".Lfa_hidle:\n"
    "s_waitcnt vmcnt(0)\n"
    "s_barrier\n"
    "s_mul_hi_u32 s34, s26, 1284865837\n"
    "s_mul_i32 s35, s26, 1481765933\n"
    "s_add_u32 s34, s34, s35\n"
    "s_mul_i32 s35, s27, 1284865837\n"
    "s_add_u32 s34, s34, s35\n"
    "s_mul_i32 s26, s26, 1284865837\n"
    "s_add_u32 s26, s26, 4150755663\n"
    "s_addc_u32 s27, s34, 335903614\n"
    "s_cmp_ge_u32 s2, 8\n"
    "s_cbranch_scc1 .Lfa_hwait\n"
    "s_cmp_eq_u32 s18, 7\n"
    "s_cbranch_scc1 .Lfa_hwait\n"
    ".Lfa_lw_15:\n"
    "s_sleep 4\n"
    "ds_read_b32 v10, v11\n"
    "s_sub_u32 s62, s62, 1\n"
    "s_cselect_b32 s62, 0, s62\n"
    "s_waitcnt lgkmcnt(0)\n"
    "s_cbranch_scc1 .Lfa_lwd_16\n"
    "v_cmp_gt_u32 vcc, 4, v10\n"
    "s_cbranch_vccnz .Lfa_lw_15\n"
    ".Lfa_lwd_16:\n"
    "s_cmp_eq_u32 s18, 6\n"
    "s_cbranch_scc1 .Lfa_hagg\n"
    "s_cmp_lg_u32 s2, 0\n"
    "s_cbranch_scc1 .Lfa_hwait\n"
    "s_mov_b32 s44, 4096\n"
    "s_sleep 40\n"
    "s_mov_b32 s60, 65536\n"
    "s_mov_b64 exec, 0xff\n"
    ".Lfa_fp_17:\n"
    "buffer_load_dwordx2 v[248:249], v3, s[36:39], s44 offen sc1\n"
    "s_sub_u32 s60, s60, 1\n"
    "s_cbranch_scc1 .Lfa_fpd_18\n"
    "s_waitcnt vmcnt(0)\n"
    "v_cmp_ne_u64 vcc, s[26:27], v[248:249]\n"
    "s_cbranch_vccz .Lfa_fpd_18\n"
    "s_sleep 16\n"
    "s_branch .Lfa_fp_17\n"
    ".Lfa_fpd_18:\n"
    "s_mov_b64 exec, -1\n"
    "s_mov_b32 s44, 0\n"
    "s_branch .Lfa_hpub\n"
    ".Lfa_hagg:\n"
    "s_lshl_b32 s44, s2, 11\n"
    "s_add_u32 s44, s44, 65536\n"
    "s_mov_b32 s48, -1\n"
    "s_mov_b32 s49, 0\n"
    "v_lshrrev_b32 v250, 3, v1\n"
    "v_and_b32 v251, 7, v1\n"
    "v_lshlrev_b32 v251, 8, v251\n"
    "v_lshl_add_u32 v250, v250, 15, v251\n"
    "s_mov_b32 s60, 65536\n"
    "s_mov_b64 exec, s[48:49]\n"
    ".Lfa_fp_19:\n"
    "buffer_load_dwordx2 v[248:249], v250, s[36:39], s44 offen sc1\n"
    "s_sub_u32 s60, s60, 1\n"
    "s_cbranch_scc1 .Lfa_fpd_20\n"
    "s_waitcnt vmcnt(0)\n"
    "v_cmp_ne_u64 vcc, s[26:27], v[248:249]\n"
    "s_cbranch_vccz .Lfa_fpd_20\n"
    "s_sleep 16\n"
    "s_branch .Lfa_fp_19\n"
    ".Lfa_fpd_20:\n"
    "s_mov_b64 exec, -1\n"
    "s_lshl_b32 s44, s2, 7\n"
    "s_add_u32 s44, s44, 4096\n"
    ".Lfa_hpub:\n"
    "v_mov_b32 v252, s26\n"
    "v_mov_b32 v253, s27\n"
    "v_mov_b32 v254, s26\n"
    "v_mov_b32 v255, s27\n"
    "s_waitcnt vmcnt(0)\n"
    "s_mov_b64 exec, 0xff\n"
    "buffer_store_dwordx4 v[252:255], v2, s[36:39], s44 offen sc1\n"
    "s_mov_b64 exec, -1\n"
    ".Lfa_hwait:\n"
    ".Lfa_lw_21:\n"
    "s_sleep 4\n"
    "ds_read_b32 v12, v13\n"
    "s_sub_u32 s62, s62, 1\n"
    "s_cselect_b32 s62, 0, s62\n"
    "s_waitcnt lgkmcnt(0)\n"
    "s_cbranch_scc1 .Lfa_lwd_22\n"
    "v_cmp_gt_u32 vcc, 10, v12\n"
    "s_cbranch_vccnz .Lfa_lw_21\n"
    ".Lfa_lwd_22:\n"
    ".Lfa_hout:\n"
    "v_bfe_u32 v244, v1, 2, 2\n"
    "v_lshrrev_b32 v245, 5, v1\n"
    "v_lshl_add_u32 v244, v245, 2, v244\n"
    "v_bfe_u32 v245, v1, 4, 1\n"
    "v_lshlrev_b32 v245, 5, v245\n"
    "v_lshl_add_u32 v244, v244, 6, v245\n"
    "v_and_b32 v245, 1, v1\n"
    "v_bfe_u32 v246, v1, 1, 1\n"
    "v_lshl_add_u32 v245, v245, 1, v246\n"
    "v_lshl_add_u32 v14, v245, 3, v244\n"
    "s_and_b32 s52, s20, 3\n"
    "s_lshr_b32 s35, s20, 4\n"
    "s_lshl_b32 s35, s35, 2\n"
    "s_add_u32 s35, s35, s33\n"
    "s_lshl_b32 s35, s35, 3\n"
    "s_add_u32 s35, s35, s19\n"
    "s_lshl_b32 s56, s35, 11\n"
    "s_add_u32 s56, s56, 327680\n"
    "s_lshl_b32 s35, s35, 15\n"
    "s_add_u32 s35, s35, 8388608\n"
    "s_add_u32 s34, s52, 0\n"
    "s_and_b32 s34, s34, 3\n"
    "s_lshl_b32 s34, s34, 13\n"
    "s_add_u32 s44, s34, s35\n"
    "s_add_u32 s45, s44, 4096\n"
    "s_add_u32 s34, s52, 1\n"
    "s_and_b32 s34, s34, 3\n"
    "s_lshl_b32 s34, s34, 13\n"
    "s_add_u32 s46, s34, s35\n"
    "s_add_u32 s47, s46, 4096\n"
    "s_add_u32 s34, s52, 2\n"
    "s_and_b32 s34, s34, 3\n"
    "s_lshl_b32 s34, s34, 13\n"
    "s_add_u32 s48, s34, s35\n"
    "s_add_u32 s49, s48, 4096\n"
    "s_add_u32 s34, s52, 3\n"
    "s_and_b32 s34, s34, 3\n"
    "s_lshl_b32 s34, s34, 13\n"
    "s_add_u32 s50, s34, s35\n"
    "s_add_u32 s51, s50, 4096\n"
    "s_lshl_b32 s52, s52, 14\n"
    "v_add_u32 v15, s52, v14\n"
    "ds_read_b64_tr_b16 v[176:177], v15 offset:0\n"
    "ds_read_b64_tr_b16 v[178:179], v15 offset:1024\n"
    "ds_read_b64_tr_b16 v[180:181], v15 offset:512\n"
    "ds_read_b64_tr_b16 v[182:183], v15 offset:1536\n"
    "ds_read_b64_tr_b16 v[184:185], v15 offset:2048\n"
    "ds_read_b64_tr_b16 v[186:187], v15 offset:3072\n"
    "ds_read_b64_tr_b16 v[188:189], v15 offset:2560\n"
    "ds_read_b64_tr_b16 v[190:191], v15 offset:3584\n"
    "ds_read_b64_tr_b16 v[192:193], v15 offset:4096\n"
    "ds_read_b64_tr_b16 v[194:195], v15 offset:5120\n"
    "ds_read_b64_tr_b16 v[196:197], v15 offset:4608\n"
    "ds_read_b64_tr_b16 v[198:199], v15 offset:5632\n"
    "ds_read_b64_tr_b16 v[200:201], v15 offset:6144\n"
    "ds_read_b64_tr_b16 v[202:203], v15 offset:7168\n"
    "ds_read_b64_tr_b16 v[204:205], v15 offset:6656\n"
    "ds_read_b64_tr_b16 v[206:207], v15 offset:7680\n"
    "s_lshl_b32 s53, s20, 2\n"
    "s_add_u32 s53, s53, s33\n"
    "s_lshl_b32 s57, s53, 3\n"
    "s_add_u32 s57, s57, s19\n"
    "s_lshl_b32 s57, s57, 7\n"
    "s_add_u32 s57, s57, 458752\n"
    "s_lshl_b32 s58, s20, 2\n"
    "s_lshr_b32 s34, s19, 1\n"
    "s_add_u32 s58, s58, s34\n"
    "s_lshl_b32 s58, s58, 10\n"
    "s_add_u32 s58, s58, 458752\n"
    "s_lshl_b32 s53, s53, 5\n"
    "s_add_u32 s53, s53, s19\n"
    "s_lshl_b32 s53, s53, 10\n"
    "s_add_u32 s53, s53, 4194304\n"
    "s_lshl_b32 s54, s20, 3\n"
    "s_add_u32 s54, s54, s19\n"
    "s_lshl_b32 s54, s54, 1\n"
    "s_add_u32 s54, s54, s33\n"
    "s_lshl_b32 s54, s54, 13\n"
    "s_add_u32 s54, s54, 4194304\n"
    "s_add_u32 s55, s54, 4096\n"
    "s_lshr_b32 s61, s20, 4\n"
    "s_lshl_b32 s61, s61, 2\n"
    "s_lshr_b32 s34, s19, 1\n"
    "s_add_u32 s61, s61, s34\n"
    "s_lshl_b32 s61, s61, 16\n"
    "s_and_b32 s34, s20, 15\n"
    "s_lshl_b32 s34, s34, 7\n"
    "s_add_u32 s61, s61, s34\n"
    "s_and_b32 s34, s19, 1\n"
    "s_lshl_b32 s34, s34, 6\n"
    "s_add_u32 s61, s61, s34\n"
    "s_lshl_b32 s34, s33, 5\n"
    "s_add_u32 s61, s61, s34\n"
    "v_and_b32 v248, 31, v1\n"
    "v_lshlrev_b32 v248, 11, v248\n"
    "v_lshrrev_b32 v249, 5, v1\n"
    "v_lshl_add_u32 v248, v249, 4, v248\n"
    "s_mov_b32 s60, 65536\n"
    "s_mov_b32 s40, s14\n"
    "s_and_b32 s41, s15, 0xffff\n"
    "s_mov_b32 s42, 524288\n"
    "s_mov_b32 s43, 0x20000\n"
    "s_waitcnt vmcnt(0)\n"
    "s_barrier\n"
    "s_mov_b32 s60, 65536\n"
    "s_mov_b64 exec, 0xffff\n"
    ".Lfa_fp_23:\n"
    "buffer_load_dwordx2 v[250:251], v3, s[36:39], s56 offen sc1\n"
    "s_sub_u32 s60, s60, 1\n"
    "s_cbranch_scc1 .Lfa_fpd_24\n"
    "s_waitcnt vmcnt(0)\n"
    "v_cmp_ne_u64 vcc, s[26:27], v[250:251]\n"
    "s_cbranch_vccnz .Lfa_fp_23\n"
    ".Lfa_fpd_24:\n"
    "s_mov_b64 exec, -1\n"
    "buffer_load_dwordx4 v[16:19], v2, s[36:39], s44 offen offset:0 sc1\n"
    "buffer_load_dwordx4 v[20:23], v2, s[36:39], s44 offen offset:1024 sc1\n"
    "buffer_load_dwordx4 v[24:27], v2, s[36:39], s44 offen offset:2048 sc1\n"
    "buffer_load_dwordx4 v[28:31], v2, s[36:39], s44 offen offset:3072 sc1\n"
    "buffer_load_dwordx4 v[32:35], v2, s[36:39], s45 offen offset:0 sc1\n"
    "buffer_load_dwordx4 v[36:39], v2, s[36:39], s45 offen offset:1024 sc1\n"
    "buffer_load_dwordx4 v[40:43], v2, s[36:39], s45 offen offset:2048 sc1\n"
    "buffer_load_dwordx4 v[44:47], v2, s[36:39], s45 offen offset:3072 sc1\n"
    "buffer_load_dwordx4 v[48:51], v2, s[36:39], s46 offen offset:0 sc1\n"
    "buffer_load_dwordx4 v[52:55], v2, s[36:39], s46 offen offset:1024 sc1\n"
    "buffer_load_dwordx4 v[56:59], v2, s[36:39], s46 offen offset:2048 sc1\n"
    "buffer_load_dwordx4 v[60:63], v2, s[36:39], s46 offen offset:3072 sc1\n"
    "buffer_load_dwordx4 v[64:67], v2, s[36:39], s47 offen offset:0 sc1\n"
    "buffer_load_dwordx4 v[68:71], v2, s[36:39], s47 offen offset:1024 sc1\n"
    "buffer_load_dwordx4 v[72:75], v2, s[36:39], s47 offen offset:2048 sc1\n"
    "buffer_load_dwordx4 v[76:79], v2, s[36:39], s47 offen offset:3072 sc1\n"
    "buffer_load_dwordx4 v[80:83], v2, s[36:39], s48 offen offset:0 sc1\n"
    "buffer_load_dwordx4 v[84:87], v2, s[36:39], s48 offen offset:1024 sc1\n"
    "buffer_load_dwordx4 v[88:91], v2, s[36:39], s48 offen offset:2048 sc1\n"
    "buffer_load_dwordx4 v[92:95], v2, s[36:39], s48 offen offset:3072 sc1\n"
    "buffer_load_dwordx4 v[96:99], v2, s[36:39], s49 offen offset:0 sc1\n"
    "buffer_load_dwordx4 v[100:103], v2, s[36:39], s49 offen offset:1024 sc1\n"
    "buffer_load_dwordx4 v[104:107], v2, s[36:39], s49 offen offset:2048 sc1\n"
    "buffer_load_dwordx4 v[108:111], v2, s[36:39], s49 offen offset:3072 sc1\n"
    "buffer_load_dwordx4 v[112:115], v2, s[36:39], s50 offen offset:0 sc1\n"
    "buffer_load_dwordx4 v[116:119], v2, s[36:39], s50 offen offset:1024 sc1\n"
    "buffer_load_dwordx4 v[120:123], v2, s[36:39], s50 offen offset:2048 sc1\n"
    "buffer_load_dwordx4 v[124:127], v2, s[36:39], s50 offen offset:3072 sc1\n"
    "buffer_load_dwordx4 v[128:131], v2, s[36:39], s51 offen offset:0 sc1\n"
    "buffer_load_dwordx4 v[132:135], v2, s[36:39], s51 offen offset:1024 sc1\n"
    "buffer_load_dwordx4 v[136:139], v2, s[36:39], s51 offen offset:2048 sc1\n"
    "buffer_load_dwordx4 v[140:143], v2, s[36:39], s51 offen offset:3072 sc1\n"
    "s_waitcnt lgkmcnt(0)\n"
    "s_waitcnt vmcnt(30)\n"
    "v_mfma_f32_32x32x16_bf16 v[144:159], v[176:179], v[16:19], 0\n"
    "ds_read_b64_tr_b16 v[208:209], v15 offset:8192\n"
    "ds_read_b64_tr_b16 v[210:211], v15 offset:9216\n"
    "ds_read_b64_tr_b16 v[212:213], v15 offset:8704\n"
    "v_mfma_f32_32x32x16_bf16 v[160:175], v[180:183], v[16:19], 0\n"
    "ds_read_b64_tr_b16 v[214:215], v15 offset:9728\n"
    "ds_read_b64_tr_b16 v[216:217], v15 offset:10240\n"
    "ds_read_b64_tr_b16 v[218:219], v15 offset:11264\n"
    "v_mfma_f32_32x32x16_bf16 v[144:159], v[184:187], v[20:23], v[144:159]\n"
    "ds_read_b64_tr_b16 v[220:221], v15 offset:10752\n"
    "ds_read_b64_tr_b16 v[222:223], v15 offset:11776\n"
    "ds_read_b64_tr_b16 v[224:225], v15 offset:12288\n"
    "v_mfma_f32_32x32x16_bf16 v[160:175], v[188:191], v[20:23], v[160:175]\n"
    "ds_read_b64_tr_b16 v[226:227], v15 offset:13312\n"
    "ds_read_b64_tr_b16 v[228:229], v15 offset:12800\n"
    "ds_read_b64_tr_b16 v[230:231], v15 offset:13824\n"
    "s_waitcnt vmcnt(28)\n"
    "v_mfma_f32_32x32x16_bf16 v[144:159], v[192:195], v[24:27], v[144:159]\n"
    "ds_read_b64_tr_b16 v[232:233], v15 offset:14336\n"
    "ds_read_b64_tr_b16 v[234:235], v15 offset:15360\n"
    "ds_read_b64_tr_b16 v[236:237], v15 offset:14848\n"
    "v_mfma_f32_32x32x16_bf16 v[160:175], v[196:199], v[24:27], v[160:175]\n"
    "ds_read_b64_tr_b16 v[238:239], v15 offset:15872\n"
    "v_mfma_f32_32x32x16_bf16 v[144:159], v[200:203], v[28:31], v[144:159]\n"
    "v_mfma_f32_32x32x16_bf16 v[160:175], v[204:207], v[28:31], v[160:175]\n"
    "s_waitcnt lgkmcnt(0)\n"
    "s_add_u32 s52, s52, 16384\n"
    "s_and_b32 s52, s52, 65535\n"
    "v_add_u32 v15, s52, v14\n"
    "s_waitcnt vmcnt(26)\n"
    "v_mfma_f32_32x32x16_bf16 v[144:159], v[208:211], v[32:35], v[144:159]\n"
    "ds_read_b64_tr_b16 v[176:177], v15 offset:0\n"
    "ds_read_b64_tr_b16 v[178:179], v15 offset:1024\n"
    "ds_read_b64_tr_b16 v[180:181], v15 offset:512\n"
    "v_mfma_f32_32x32x16_bf16 v[160:175], v[212:215], v[32:35], v[160:175]\n"
    "ds_read_b64_tr_b16 v[182:183], v15 offset:1536\n"
    "ds_read_b64_tr_b16 v[184:185], v15 offset:2048\n"
    "ds_read_b64_tr_b16 v[186:187], v15 offset:3072\n"
    "v_mfma_f32_32x32x16_bf16 v[144:159], v[216:219], v[36:39], v[144:159]\n"
    "ds_read_b64_tr_b16 v[188:189], v15 offset:2560\n"
    "ds_read_b64_tr_b16 v[190:191], v15 offset:3584\n"
    "ds_read_b64_tr_b16 v[192:193], v15 offset:4096\n"
    "v_mfma_f32_32x32x16_bf16 v[160:175], v[220:223], v[36:39], v[160:175]\n"
    "ds_read_b64_tr_b16 v[194:195], v15 offset:5120\n"
    "ds_read_b64_tr_b16 v[196:197], v15 offset:4608\n"
    "ds_read_b64_tr_b16 v[198:199], v15 offset:5632\n"
    "s_waitcnt vmcnt(24)\n"
    "v_mfma_f32_32x32x16_bf16 v[144:159], v[224:227], v[40:43], v[144:159]\n"
    "ds_read_b64_tr_b16 v[200:201], v15 offset:6144\n"
    "ds_read_b64_tr_b16 v[202:203], v15 offset:7168\n"
    "ds_read_b64_tr_b16 v[204:205], v15 offset:6656\n"
    "v_mfma_f32_32x32x16_bf16 v[160:175], v[228:231], v[40:43], v[160:175]\n"
    "ds_read_b64_tr_b16 v[206:207], v15 offset:7680\n"
    "v_mfma_f32_32x32x16_bf16 v[144:159], v[232:235], v[44:47], v[144:159]\n"
    "v_mfma_f32_32x32x16_bf16 v[160:175], v[236:239], v[44:47], v[160:175]\n"
    "s_waitcnt lgkmcnt(0)\n"
    "s_waitcnt vmcnt(22)\n"
    "v_mfma_f32_32x32x16_bf16 v[144:159], v[176:179], v[48:51], v[144:159]\n"
    "ds_read_b64_tr_b16 v[208:209], v15 offset:8192\n"
    "ds_read_b64_tr_b16 v[210:211], v15 offset:9216\n"
    "ds_read_b64_tr_b16 v[212:213], v15 offset:8704\n"
    "v_mfma_f32_32x32x16_bf16 v[160:175], v[180:183], v[48:51], v[160:175]\n"
    "ds_read_b64_tr_b16 v[214:215], v15 offset:9728\n"
    "ds_read_b64_tr_b16 v[216:217], v15 offset:10240\n"
    "ds_read_b64_tr_b16 v[218:219], v15 offset:11264\n"
    "v_mfma_f32_32x32x16_bf16 v[144:159], v[184:187], v[52:55], v[144:159]\n"
    "ds_read_b64_tr_b16 v[220:221], v15 offset:10752\n"
    "ds_read_b64_tr_b16 v[222:223], v15 offset:11776\n"
    "ds_read_b64_tr_b16 v[224:225], v15 offset:12288\n"
    "v_mfma_f32_32x32x16_bf16 v[160:175], v[188:191], v[52:55], v[160:175]\n"
    "ds_read_b64_tr_b16 v[226:227], v15 offset:13312\n"
    "ds_read_b64_tr_b16 v[228:229], v15 offset:12800\n"
    "ds_read_b64_tr_b16 v[230:231], v15 offset:13824\n"
    "s_waitcnt vmcnt(20)\n"
    "v_mfma_f32_32x32x16_bf16 v[144:159], v[192:195], v[56:59], v[144:159]\n"
    "ds_read_b64_tr_b16 v[232:233], v15 offset:14336\n"
    "ds_read_b64_tr_b16 v[234:235], v15 offset:15360\n"
    "ds_read_b64_tr_b16 v[236:237], v15 offset:14848\n"
    "v_mfma_f32_32x32x16_bf16 v[160:175], v[196:199], v[56:59], v[160:175]\n"
    "ds_read_b64_tr_b16 v[238:239], v15 offset:15872\n"
    "v_mfma_f32_32x32x16_bf16 v[144:159], v[200:203], v[60:63], v[144:159]\n"
    "v_mfma_f32_32x32x16_bf16 v[160:175], v[204:207], v[60:63], v[160:175]\n"
    "s_waitcnt lgkmcnt(0)\n"
    "s_add_u32 s52, s52, 16384\n"
    "s_and_b32 s52, s52, 65535\n"
    "v_add_u32 v15, s52, v14\n"
    "s_waitcnt vmcnt(18)\n"
    "v_mfma_f32_32x32x16_bf16 v[144:159], v[208:211], v[64:67], v[144:159]\n"
    "ds_read_b64_tr_b16 v[176:177], v15 offset:0\n"
    "ds_read_b64_tr_b16 v[178:179], v15 offset:1024\n"
    "ds_read_b64_tr_b16 v[180:181], v15 offset:512\n"
    "v_mfma_f32_32x32x16_bf16 v[160:175], v[212:215], v[64:67], v[160:175]\n"
    "ds_read_b64_tr_b16 v[182:183], v15 offset:1536\n"
    "ds_read_b64_tr_b16 v[184:185], v15 offset:2048\n"
    "ds_read_b64_tr_b16 v[186:187], v15 offset:3072\n"
    "v_mfma_f32_32x32x16_bf16 v[144:159], v[216:219], v[68:71], v[144:159]\n"
    "ds_read_b64_tr_b16 v[188:189], v15 offset:2560\n"
    "ds_read_b64_tr_b16 v[190:191], v15 offset:3584\n"
    "ds_read_b64_tr_b16 v[192:193], v15 offset:4096\n"
    "v_mfma_f32_32x32x16_bf16 v[160:175], v[220:223], v[68:71], v[160:175]\n"
    "ds_read_b64_tr_b16 v[194:195], v15 offset:5120\n"
    "ds_read_b64_tr_b16 v[196:197], v15 offset:4608\n"
    "ds_read_b64_tr_b16 v[198:199], v15 offset:5632\n"
    "s_waitcnt vmcnt(16)\n"
    "v_mfma_f32_32x32x16_bf16 v[144:159], v[224:227], v[72:75], v[144:159]\n"
    "ds_read_b64_tr_b16 v[200:201], v15 offset:6144\n"
    "ds_read_b64_tr_b16 v[202:203], v15 offset:7168\n"
    "ds_read_b64_tr_b16 v[204:205], v15 offset:6656\n"
    "v_mfma_f32_32x32x16_bf16 v[160:175], v[228:231], v[72:75], v[160:175]\n"
    "ds_read_b64_tr_b16 v[206:207], v15 offset:7680\n"
    "v_mfma_f32_32x32x16_bf16 v[144:159], v[232:235], v[76:79], v[144:159]\n"
    "v_mfma_f32_32x32x16_bf16 v[160:175], v[236:239], v[76:79], v[160:175]\n"
    "s_waitcnt lgkmcnt(0)\n"
    "s_waitcnt vmcnt(14)\n"
    "v_mfma_f32_32x32x16_bf16 v[144:159], v[176:179], v[80:83], v[144:159]\n"
    "ds_read_b64_tr_b16 v[208:209], v15 offset:8192\n"
    "ds_read_b64_tr_b16 v[210:211], v15 offset:9216\n"
    "ds_read_b64_tr_b16 v[212:213], v15 offset:8704\n"
    "v_mfma_f32_32x32x16_bf16 v[160:175], v[180:183], v[80:83], v[160:175]\n"
    "ds_read_b64_tr_b16 v[214:215], v15 offset:9728\n"
    "ds_read_b64_tr_b16 v[216:217], v15 offset:10240\n"
    "ds_read_b64_tr_b16 v[218:219], v15 offset:11264\n"
    "v_mfma_f32_32x32x16_bf16 v[144:159], v[184:187], v[84:87], v[144:159]\n"
    "ds_read_b64_tr_b16 v[220:221], v15 offset:10752\n"
    "ds_read_b64_tr_b16 v[222:223], v15 offset:11776\n"
    "ds_read_b64_tr_b16 v[224:225], v15 offset:12288\n"
    "v_mfma_f32_32x32x16_bf16 v[160:175], v[188:191], v[84:87], v[160:175]\n"
    "ds_read_b64_tr_b16 v[226:227], v15 offset:13312\n"
    "ds_read_b64_tr_b16 v[228:229], v15 offset:12800\n"
    "ds_read_b64_tr_b16 v[230:231], v15 offset:13824\n"
    "s_waitcnt vmcnt(12)\n"
    "v_mfma_f32_32x32x16_bf16 v[144:159], v[192:195], v[88:91], v[144:159]\n"
    "ds_read_b64_tr_b16 v[232:233], v15 offset:14336\n"
    "ds_read_b64_tr_b16 v[234:235], v15 offset:15360\n"
    "ds_read_b64_tr_b16 v[236:237], v15 offset:14848\n"
    "v_mfma_f32_32x32x16_bf16 v[160:175], v[196:199], v[88:91], v[160:175]\n"
    "ds_read_b64_tr_b16 v[238:239], v15 offset:15872\n"
    "v_mfma_f32_32x32x16_bf16 v[144:159], v[200:203], v[92:95], v[144:159]\n"
    "v_mfma_f32_32x32x16_bf16 v[160:175], v[204:207], v[92:95], v[160:175]\n"
    "s_waitcnt lgkmcnt(0)\n"
    "s_add_u32 s52, s52, 16384\n"
    "s_and_b32 s52, s52, 65535\n"
    "v_add_u32 v15, s52, v14\n"
    "s_waitcnt vmcnt(10)\n"
    "v_mfma_f32_32x32x16_bf16 v[144:159], v[208:211], v[96:99], v[144:159]\n"
    "ds_read_b64_tr_b16 v[176:177], v15 offset:0\n"
    "ds_read_b64_tr_b16 v[178:179], v15 offset:1024\n"
    "ds_read_b64_tr_b16 v[180:181], v15 offset:512\n"
    "v_mfma_f32_32x32x16_bf16 v[160:175], v[212:215], v[96:99], v[160:175]\n"
    "ds_read_b64_tr_b16 v[182:183], v15 offset:1536\n"
    "ds_read_b64_tr_b16 v[184:185], v15 offset:2048\n"
    "ds_read_b64_tr_b16 v[186:187], v15 offset:3072\n"
    "v_mfma_f32_32x32x16_bf16 v[144:159], v[216:219], v[100:103], v[144:159]\n"
    "ds_read_b64_tr_b16 v[188:189], v15 offset:2560\n"
    "ds_read_b64_tr_b16 v[190:191], v15 offset:3584\n"
    "ds_read_b64_tr_b16 v[192:193], v15 offset:4096\n"
    "v_mfma_f32_32x32x16_bf16 v[160:175], v[220:223], v[100:103], v[160:175]\n"
    "ds_read_b64_tr_b16 v[194:195], v15 offset:5120\n"
    "ds_read_b64_tr_b16 v[196:197], v15 offset:4608\n"
    "ds_read_b64_tr_b16 v[198:199], v15 offset:5632\n"
    "s_waitcnt vmcnt(8)\n"
    "v_mfma_f32_32x32x16_bf16 v[144:159], v[224:227], v[104:107], v[144:159]\n"
    "ds_read_b64_tr_b16 v[200:201], v15 offset:6144\n"
    "ds_read_b64_tr_b16 v[202:203], v15 offset:7168\n"
    "ds_read_b64_tr_b16 v[204:205], v15 offset:6656\n"
    "v_mfma_f32_32x32x16_bf16 v[160:175], v[228:231], v[104:107], v[160:175]\n"
    "ds_read_b64_tr_b16 v[206:207], v15 offset:7680\n"
    "v_mfma_f32_32x32x16_bf16 v[144:159], v[232:235], v[108:111], v[144:159]\n"
    "v_mfma_f32_32x32x16_bf16 v[160:175], v[236:239], v[108:111], v[160:175]\n"
    "s_waitcnt lgkmcnt(0)\n"
    "s_waitcnt vmcnt(6)\n"
    "v_mfma_f32_32x32x16_bf16 v[144:159], v[176:179], v[112:115], v[144:159]\n"
    "ds_read_b64_tr_b16 v[208:209], v15 offset:8192\n"
    "ds_read_b64_tr_b16 v[210:211], v15 offset:9216\n"
    "ds_read_b64_tr_b16 v[212:213], v15 offset:8704\n"
    "v_mfma_f32_32x32x16_bf16 v[160:175], v[180:183], v[112:115], v[160:175]\n"
    "ds_read_b64_tr_b16 v[214:215], v15 offset:9728\n"
    "ds_read_b64_tr_b16 v[216:217], v15 offset:10240\n"
    "ds_read_b64_tr_b16 v[218:219], v15 offset:11264\n"
    "v_mfma_f32_32x32x16_bf16 v[144:159], v[184:187], v[116:119], v[144:159]\n"
    "ds_read_b64_tr_b16 v[220:221], v15 offset:10752\n"
    "ds_read_b64_tr_b16 v[222:223], v15 offset:11776\n"
    "ds_read_b64_tr_b16 v[224:225], v15 offset:12288\n"
    "v_mfma_f32_32x32x16_bf16 v[160:175], v[188:191], v[116:119], v[160:175]\n"
    "ds_read_b64_tr_b16 v[226:227], v15 offset:13312\n"
    "ds_read_b64_tr_b16 v[228:229], v15 offset:12800\n"
    "ds_read_b64_tr_b16 v[230:231], v15 offset:13824\n"
    "s_waitcnt vmcnt(4)\n"
    "v_mfma_f32_32x32x16_bf16 v[144:159], v[192:195], v[120:123], v[144:159]\n"
    "ds_read_b64_tr_b16 v[232:233], v15 offset:14336\n"
    "ds_read_b64_tr_b16 v[234:235], v15 offset:15360\n"
    "ds_read_b64_tr_b16 v[236:237], v15 offset:14848\n"
    "v_mfma_f32_32x32x16_bf16 v[160:175], v[196:199], v[120:123], v[160:175]\n"
    "ds_read_b64_tr_b16 v[238:239], v15 offset:15872\n"
    "v_mfma_f32_32x32x16_bf16 v[144:159], v[200:203], v[124:127], v[144:159]\n"
    "v_mfma_f32_32x32x16_bf16 v[160:175], v[204:207], v[124:127], v[160:175]\n"
    "s_waitcnt lgkmcnt(0)\n"
    "s_waitcnt vmcnt(2)\n"
    "v_mfma_f32_32x32x16_bf16 v[144:159], v[208:211], v[128:131], v[144:159]\n"
    "v_mfma_f32_32x32x16_bf16 v[160:175], v[212:215], v[128:131], v[160:175]\n"
    "v_mfma_f32_32x32x16_bf16 v[144:159], v[216:219], v[132:135], v[144:159]\n"
    "v_mfma_f32_32x32x16_bf16 v[160:175], v[220:223], v[132:135], v[160:175]\n"
    "s_waitcnt vmcnt(0)\n"
    "v_mfma_f32_32x32x16_bf16 v[144:159], v[224:227], v[136:139], v[144:159]\n"
    "v_mfma_f32_32x32x16_bf16 v[160:175], v[228:231], v[136:139], v[160:175]\n"
    "v_mfma_f32_32x32x16_bf16 v[144:159], v[232:235], v[140:143], v[144:159]\n"
    "v_mfma_f32_32x32x16_bf16 v[160:175], v[236:239], v[140:143], v[160:175]\n"
    "s_nop 7\n"
    "s_nop 3\n"
    "v_cvt_pk_bf16_f32 v16, v144, v145\n"
    "v_cvt_pk_bf16_f32 v17, v146, v147\n"
    "v_cvt_pk_bf16_f32 v18, v148, v149\n"
    "v_cvt_pk_bf16_f32 v19, v150, v151\n"
    "buffer_store_dwordx4 v[16:19], v2, s[36:39], s53 offen sc1\n"
    "v_cvt_pk_bf16_f32 v20, v152, v153\n"
    "v_cvt_pk_bf16_f32 v21, v154, v155\n"
    "v_cvt_pk_bf16_f32 v22, v156, v157\n"
    "v_cvt_pk_bf16_f32 v23, v158, v159\n"
    "s_add_u32 s53, s53, 8192\n"
    "buffer_store_dwordx4 v[20:23], v2, s[36:39], s53 offen sc1\n"
    "v_cvt_pk_bf16_f32 v24, v160, v161\n"
    "v_cvt_pk_bf16_f32 v25, v162, v163\n"
    "v_cvt_pk_bf16_f32 v26, v164, v165\n"
    "v_cvt_pk_bf16_f32 v27, v166, v167\n"
    "s_add_u32 s53, s53, 8192\n"
    "buffer_store_dwordx4 v[24:27], v2, s[36:39], s53 offen sc1\n"
    "v_cvt_pk_bf16_f32 v28, v168, v169\n"
    "v_cvt_pk_bf16_f32 v29, v170, v171\n"
    "v_cvt_pk_bf16_f32 v30, v172, v173\n"
    "v_cvt_pk_bf16_f32 v31, v174, v175\n"
    "s_add_u32 s53, s53, 8192\n"
    "buffer_store_dwordx4 v[28:31], v2, s[36:39], s53 offen sc1\n"
    "v_mov_b32 v252, s26\n"
    "v_mov_b32 v253, s27\n"
    "v_mov_b32 v254, s26\n"
    "v_mov_b32 v255, s27\n"
    "s_waitcnt vmcnt(0)\n"
    "s_mov_b64 exec, 0xff\n"
    "buffer_store_dwordx4 v[252:255], v2, s[36:39], s57 offen sc1\n"
    "s_mov_b64 exec, -1\n"
    "s_cmp_ge_u32 s33, 2\n"
    "s_cbranch_scc1 .Lfa_hend\n"
    "s_mov_b32 s60, 65536\n"
    "s_mov_b64 exec, 0xff\n"
    ".Lfa_fp_25:\n"
    "buffer_load_dwordx2 v[250:251], v3, s[36:39], s58 offen sc1\n"
    "s_sub_u32 s60, s60, 1\n"
    "s_cbranch_scc1 .Lfa_fpd_26\n"
    "s_waitcnt vmcnt(0)\n"
    "v_cmp_ne_u64 vcc, s[26:27], v[250:251]\n"
    "s_cbranch_vccnz .Lfa_fp_25\n"
    ".Lfa_fpd_26:\n"
    "s_mov_b64 exec, -1\n"
    "buffer_load_dwordx4 v[48:51], v2, s[36:39], s54 offen offset:0 sc1\n"
    "buffer_load_dwordx4 v[52:55], v2, s[36:39], s54 offen offset:1024 sc1\n"
    "buffer_load_dwordx4 v[56:59], v2, s[36:39], s54 offen offset:2048 sc1\n"
    "buffer_load_dwordx4 v[60:63], v2, s[36:39], s54 offen offset:3072 sc1\n"
    "buffer_load_dwordx4 v[64:67], v2, s[36:39], s55 offen offset:0 sc1\n"
    "buffer_load_dwordx4 v[68:71], v2, s[36:39], s55 offen offset:1024 sc1\n"
    "buffer_load_dwordx4 v[72:75], v2, s[36:39], s55 offen offset:2048 sc1\n"
    "buffer_load_dwordx4 v[76:79], v2, s[36:39], s55 offen offset:3072 sc1\n"
    "s_waitcnt vmcnt(7)\n"
    "v_lshlrev_b32 v96, 16, v48\n"
    "v_and_b32 v97, 0xffff0000, v48\n"
    "v_lshlrev_b32 v98, 16, v49\n"
    "v_and_b32 v99, 0xffff0000, v49\n"
    "v_lshlrev_b32 v100, 16, v50\n"
    "v_and_b32 v101, 0xffff0000, v50\n"
    "v_lshlrev_b32 v102, 16, v51\n"
    "v_and_b32 v103, 0xffff0000, v51\n"
    "s_waitcnt vmcnt(6)\n"
    "v_lshlrev_b32 v104, 16, v52\n"
    "v_and_b32 v105, 0xffff0000, v52\n"
    "v_lshlrev_b32 v106, 16, v53\n"
    "v_and_b32 v107, 0xffff0000, v53\n"
    "v_lshlrev_b32 v108, 16, v54\n"
    "v_and_b32 v109, 0xffff0000, v54\n"
    "v_lshlrev_b32 v110, 16, v55\n"
    "v_and_b32 v111, 0xffff0000, v55\n"
    "v_pk_add_f32 v[96:97], v[96:97], v[104:105]\n"
    "v_pk_add_f32 v[98:99], v[98:99], v[106:107]\n"
    "v_pk_add_f32 v[100:101], v[100:101], v[108:109]\n"
    "v_pk_add_f32 v[102:103], v[102:103], v[110:111]\n"
    "s_waitcnt vmcnt(5)\n"
    "v_lshlrev_b32 v104, 16, v56\n"
    "v_and_b32 v105, 0xffff0000, v56\n"
    "v_lshlrev_b32 v106, 16, v57\n"
    "v_and_b32 v107, 0xffff0000, v57\n"
    "v_lshlrev_b32 v108, 16, v58\n"
    "v_and_b32 v109, 0xffff0000, v58\n"
    "v_lshlrev_b32 v110, 16, v59\n"
    "v_and_b32 v111, 0xffff0000, v59\n"
    "v_pk_add_f32 v[96:97], v[96:97], v[104:105]\n"
    "v_pk_add_f32 v[98:99], v[98:99], v[106:107]\n"
    "v_pk_add_f32 v[100:101], v[100:101], v[108:109]\n"
    "v_pk_add_f32 v[102:103], v[102:103], v[110:111]\n"
    "s_waitcnt vmcnt(4)\n"
    "v_lshlrev_b32 v104, 16, v60\n"
    "v_and_b32 v105, 0xffff0000, v60\n"
    "v_lshlrev_b32 v106, 16, v61\n"
    "v_and_b32 v107, 0xffff0000, v61\n"
    "v_lshlrev_b32 v108, 16, v62\n"
    "v_and_b32 v109, 0xffff0000, v62\n"
    "v_lshlrev_b32 v110, 16, v63\n"
    "v_and_b32 v111, 0xffff0000, v63\n"
    "v_pk_add_f32 v[96:97], v[96:97], v[104:105]\n"
    "v_pk_add_f32 v[98:99], v[98:99], v[106:107]\n"
    "v_pk_add_f32 v[100:101], v[100:101], v[108:109]\n"
    "v_pk_add_f32 v[102:103], v[102:103], v[110:111]\n"
    "s_waitcnt vmcnt(3)\n"
    "v_lshlrev_b32 v104, 16, v64\n"
    "v_and_b32 v105, 0xffff0000, v64\n"
    "v_lshlrev_b32 v106, 16, v65\n"
    "v_and_b32 v107, 0xffff0000, v65\n"
    "v_lshlrev_b32 v108, 16, v66\n"
    "v_and_b32 v109, 0xffff0000, v66\n"
    "v_lshlrev_b32 v110, 16, v67\n"
    "v_and_b32 v111, 0xffff0000, v67\n"
    "v_pk_add_f32 v[96:97], v[96:97], v[104:105]\n"
    "v_pk_add_f32 v[98:99], v[98:99], v[106:107]\n"
    "v_pk_add_f32 v[100:101], v[100:101], v[108:109]\n"
    "v_pk_add_f32 v[102:103], v[102:103], v[110:111]\n"
    "s_waitcnt vmcnt(2)\n"
    "v_lshlrev_b32 v104, 16, v68\n"
    "v_and_b32 v105, 0xffff0000, v68\n"
    "v_lshlrev_b32 v106, 16, v69\n"
    "v_and_b32 v107, 0xffff0000, v69\n"
    "v_lshlrev_b32 v108, 16, v70\n"
    "v_and_b32 v109, 0xffff0000, v70\n"
    "v_lshlrev_b32 v110, 16, v71\n"
    "v_and_b32 v111, 0xffff0000, v71\n"
    "v_pk_add_f32 v[96:97], v[96:97], v[104:105]\n"
    "v_pk_add_f32 v[98:99], v[98:99], v[106:107]\n"
    "v_pk_add_f32 v[100:101], v[100:101], v[108:109]\n"
    "v_pk_add_f32 v[102:103], v[102:103], v[110:111]\n"
    "s_waitcnt vmcnt(1)\n"
    "v_lshlrev_b32 v104, 16, v72\n"
    "v_and_b32 v105, 0xffff0000, v72\n"
    "v_lshlrev_b32 v106, 16, v73\n"
    "v_and_b32 v107, 0xffff0000, v73\n"
    "v_lshlrev_b32 v108, 16, v74\n"
    "v_and_b32 v109, 0xffff0000, v74\n"
    "v_lshlrev_b32 v110, 16, v75\n"
    "v_and_b32 v111, 0xffff0000, v75\n"
    "v_pk_add_f32 v[96:97], v[96:97], v[104:105]\n"
    "v_pk_add_f32 v[98:99], v[98:99], v[106:107]\n"
    "v_pk_add_f32 v[100:101], v[100:101], v[108:109]\n"
    "v_pk_add_f32 v[102:103], v[102:103], v[110:111]\n"
    "s_waitcnt vmcnt(0)\n"
    "v_lshlrev_b32 v104, 16, v76\n"
    "v_and_b32 v105, 0xffff0000, v76\n"
    "v_lshlrev_b32 v106, 16, v77\n"
    "v_and_b32 v107, 0xffff0000, v77\n"
    "v_lshlrev_b32 v108, 16, v78\n"
    "v_and_b32 v109, 0xffff0000, v78\n"
    "v_lshlrev_b32 v110, 16, v79\n"
    "v_and_b32 v111, 0xffff0000, v79\n"
    "v_pk_add_f32 v[96:97], v[96:97], v[104:105]\n"
    "v_pk_add_f32 v[98:99], v[98:99], v[106:107]\n"
    "v_pk_add_f32 v[100:101], v[100:101], v[108:109]\n"
    "v_pk_add_f32 v[102:103], v[102:103], v[110:111]\n"
    "v_cvt_pk_bf16_f32 v104, v96, v97\n"
    "v_cvt_pk_bf16_f32 v105, v98, v99\n"
    "v_cvt_pk_bf16_f32 v106, v100, v101\n"
    "v_cvt_pk_bf16_f32 v107, v102, v103\n"
    "buffer_store_dwordx4 v[104:107], v248, s[40:43], s61 offen\n"
    ".Lfa_hend:\n"
    "s_endpgm\n"
    :
    : "{v0}"(tid), "{s2}"(bid), "{s[0:1]}"(__builtin_amdgcn_kernarg_segment_ptr()), "{s3}"(ldsbase)
    : "v1", "v2", "v3", "v4", "v5", "v6", "v7", "v8", "v9", "v10", "v11", "v12", "v13", "v14", "v15", "v16", "v17", "v18", "v19", "v20", "v21", "v22", "v23", "v24", "v25", "v26", "v27", "v28", "v29", "v30", "v31", "v32", "v33", "v34", "v35", "v36", "v37", "v38", "v39", "v40", "v41", "v42", "v43", "v44", "v45", "v46", "v47", "v48", "v49", "v50", "v51", "v52", "v53", "v54", "v55", "v56", "v57", "v58", "v59", "v60", "v61", "v62", "v63", "v64", "v65", "v66", "v67", "v68", "v69", "v70", "v71", "v72", "v73", "v74", "v75", "v76", "v77", "v78", "v79", "v80", "v81", "v82", "v83", "v84", "v85", "v86", "v87", "v88", "v89", "v90", "v91", "v92", "v93", "v94", "v95", "v96", "v97", "v98", "v99", "v100", "v101", "v102", "v103", "v104", "v105", "v106", "v107", "v108", "v109", "v110", "v111", "v112", "v113", "v114", "v115", "v116", "v117", "v118", "v119", "v120", "v121", "v122", "v123", "v124", "v125", "v126", "v127", "v128", "v129", "v130", "v131", "v132", "v133", "v134", "v135", "v136", "v137", "v138", "v139", "v140", "v141", "v142", "v143", "v144", "v145", "v146", "v147", "v148", "v149", "v150", "v151", "v152", "v153", "v154", "v155", "v156", "v157", "v158", "v159", "v160", "v161", "v162", "v163", "v164", "v165", "v166", "v167", "v168", "v169", "v170", "v171", "v172", "v173", "v174", "v175", "v176", "v177", "v178", "v179", "v180", "v181", "v182", "v183", "v184", "v185", "v186", "v187", "v188", "v189", "v190", "v191", "v192", "v193", "v194", "v195", "v196", "v197", "v198", "v199", "v200", "v201", "v202", "v203", "v204", "v205", "v206", "v207", "v208", "v209", "v210", "v211", "v212", "v213", "v214", "v215", "v216", "v217", "v218", "v219", "v220", "v221", "v222", "v223", "v224", "v225", "v226", "v227", "v228", "v229", "v230", "v231", "v232", "v233", "v234", "v235", "v236", "v237", "v238", "v239", "v240", "v241", "v242", "v243", "v244", "v245", "v246", "v247", "v248", "v249", "v250", "v251", "v252", "v253", "v254", "v255", "s4", "s5", "s6", "s7", "s8", "s9", "s10", "s11", "s12", "s13", "s14", "s15", "s16", "s17", "s18", "s19", "s20", "s21", "s22", "s23", "s24", "s25", "s26", "s27", "s28", "s29", "s30", "s31", "s33", "s34", "s35", "s36", "s37", "s38", "s39", "s40", "s41", "s42", "s43", "s44", "s45", "s46", "s47", "s48", "s49", "s50", "s51", "s52", "s53", "s54", "s55", "s56", "s57", "s58", "s59", "s60", "s61", "s62", "s63", "s64", "s65", "s66", "s67", "s68", "s69", "s70", "s71", "s72", "s73", "s74", "s75", "s76", "s77", "s78", "s79", "s80", "s81", "s82", "s83", "s84", "s85", "s86", "s87", "s88", "s89", "s90", "s91", "s92", "s93", "s94", "s95", "s96", "s97", "s98", "s99", "vcc", "scc", "memory");
}

extern "C" void kernel_launch(void* const* d_in, const int* in_sizes, int n_in, void* d_out, int out_size, void* d_ws,
                              size_t ws_size, hipStream_t stream) {
  (void)in_sizes; (void)n_in; (void)out_size; (void)ws_size;
  fused_attn_block<<<256, 512, 0, stream>>>(d_in[0], d_in[1], d_in[2], d_in[3], d_in[4], d_out, d_ws);
}
```
